```python
import torch

def _softmax(x, axis=-1):
    return torch.softmax(x, dim=axis)
def _gelu(x):
    return torch.nn.functional.gelu(x, approximate="tanh")
def _silu(x):
    return torch.nn.functional.silu(x)
def _sigmoid(x):
    return torch.sigmoid(x)
def _layernorm(x, axis=-1, eps=1e-5):
    m = x.mean(dim=axis, keepdim=True)
    v = x.var(dim=axis, keepdim=True, unbiased=False)
    return (x - m) / torch.sqrt(v + eps)
def _scatter_add(tgt, idx, src, axis=0):
    return tgt.clone().index_add_(axis, idx, src)
def _segment_sum(src, seg, n):
    out = torch.zeros((n,) + src.shape[1:], dtype=src.dtype)
    return out.index_add_(0, seg, src)
def _pad(x, pw):
    flat = tuple(v for pair in reversed(pw) for v in pair)
    return torch.nn.functional.pad(x, flat)

kernel_name = 'gated_mix__ssm_conv_s5__dense_activation_relu__prompt'

def setup_inputs():

    _w = (lambda a: (lambda t: t.to(torch.bfloat16) if t.is_floating_point() else t)(torch.as_tensor(a)))
    def _A_setup():
        B, S, D = 1, 2048, 640
        P = D
        Lambda_re = -0.5 * (lambda _x: torch.exp(torch.as_tensor(_x)))((lambda *s: torch.randn(*s, dtype=torch.bfloat16) * 0.8)(P))
        Lambda_im = (lambda *s: torch.randn(*s, dtype=torch.bfloat16) * 0.8)(P) * 1.0
        B_re = (lambda *s: torch.randn(*s, dtype=torch.bfloat16) * 0.8)(P, D) * (D ** -0.5)
        B_im = (lambda *s: torch.randn(*s, dtype=torch.bfloat16) * 0.8)(P, D) * (D ** -0.5)
        C_re = (lambda *s: torch.randn(*s, dtype=torch.bfloat16) * 0.8)(D, P) * (P ** -0.5)
        C_im = (lambda *s: torch.randn(*s, dtype=torch.bfloat16) * 0.8)(D, P) * (P ** -0.5)
        log_dt = (lambda *s: torch.randn(*s, dtype=torch.bfloat16) * 0.8)(P) * 0.3 - 2.0
        Dskip = (lambda *s: torch.randn(*s, dtype=torch.bfloat16) * 0.8)(D)
        return {
            "x": _w((lambda *s: torch.randn(*s, dtype=torch.bfloat16) * 0.05)(B, S, D)),
            "Lambda_re": _w(Lambda_re),
            "Lambda_im": _w(Lambda_im),
            "B_re": _w(B_re),
            "B_im": _w(B_im),
            "C_re": _w(C_re),
            "C_im": _w(C_im),
            "log_dt": _w(log_dt),
            "Dskip": _w(Dskip),
        }
    _A_in = _A_setup()

    def _B_setup():
        B, S, D, Dff = 1, 2048, 640, 1728
        return {
            "x": _w((lambda *s: torch.randn(*s, dtype=torch.bfloat16) * 0.05)(B, S, D)),
            "w1": _w((lambda *s: torch.randn(*s, dtype=torch.bfloat16) * 0.05)(D, Dff)),
            "w2": _w((lambda *s: torch.randn(*s, dtype=torch.bfloat16) * 0.05)(Dff, D)),
        }
    _B_in = _B_setup()

    return {
        "A_x": _A_in["x"],
        "A_Lambda_re": _A_in["Lambda_re"],
        "A_Lambda_im": _A_in["Lambda_im"],
        "A_B_re": _A_in["B_re"],
        "A_B_im": _A_in["B_im"],
        "A_C_re": _A_in["C_re"],
        "A_C_im": _A_in["C_im"],
        "A_log_dt": _A_in["log_dt"],
        "A_Dskip": _A_in["Dskip"],
        "B_x": _B_in["x"],
        "B_w1": _B_in["w1"],
        "B_w2": _B_in["w2"],
        "gate_w": _w((lambda *s: torch.randn(*s, dtype=torch.bfloat16) * 0.05)(640) * 16.0),
    }

def reference(A_x, A_Lambda_re, A_Lambda_im, A_B_re, A_B_im, A_C_re, A_C_im, A_log_dt, A_Dskip, B_x, B_w1, B_w2, gate_w):
    def _A_ref(x, Lambda_re, Lambda_im, B_re, B_im, C_re, C_im, log_dt, Dskip):
        Bsz, L, H = x.shape
        Lam = Lambda_re + 1j * Lambda_im
        Bmat = B_re + 1j * B_im
        Cmat = C_re + 1j * C_im
        dt = torch.exp(log_dt)
        Lam_bar = torch.exp(dt * Lam)
        B_bar = ((Lam_bar - 1.0) / Lam)[:, None] * Bmat
        Bu = torch.einsum("bld,pd->blp", x + 0j, B_bar)
        h = torch.zeros_like(Bu[:, 0, :])
        hs = []
        for t in range(L):
            h = Lam_bar[None, :] * h + Bu[:, t, :]
            hs.append(h)
        hseq = torch.stack(hs, 1)
        y = 2.0 * torch.einsum("blp,dp->bld", hseq, Cmat).real + x * Dskip[None, None, :]
        return y

    def _B_ref(x, w1, w2):
        h = torch.einsum("bsd,df->bsf", x, w1)
        zero = torch.zeros_like(h)
        h = torch.maximum(h, zero)
        return torch.einsum("bsf,fd->bsd", h, w2)

    x = A_x
    g = _sigmoid((x * gate_w).sum(-1, keepdims=True))
    a = _A_ref(x, A_Lambda_re, A_Lambda_im, A_B_re, A_B_im, A_C_re, A_C_im, A_log_dt, A_Dskip)
    b = _B_ref(x, B_w1, B_w2)
    return g * a + (1 - g) * b
```

```cpp
#include <hip/hip_runtime.h>
#include <hip/hip_bf16.h>
#include <stdint.h>

typedef __attribute__((ext_vector_type(8))) __bf16 bf16x8;
typedef __attribute__((ext_vector_type(4))) __bf16 bf16x4;
typedef __attribute__((ext_vector_type(16))) float f32x16;
typedef __attribute__((ext_vector_type(4))) float f32x4;
typedef __attribute__((address_space(3))) bf16x4* lds_bf16x4_ptr;

#define LSEQ 2048
#define DM 640
#define PN 640
#define FF 1728
#define CHUNK 64
#define NCHUNK (LSEQ / CHUNK)
#define KS_MLP (FF / 32)
#define KS_SSM (2 * PN / 32)
#define MT (LSEQ / 16)
#define NT (DM / 16)

#define WS_LOCAL 0
#define WS_LOCAL_SZ ((size_t)LSEQ * PN * 8)
#define WS_AGG (WS_LOCAL + WS_LOCAL_SZ)
#define WS_AGG_SZ ((size_t)NCHUNK * PN * 8)
#define WS_HIDA (WS_AGG + WS_AGG_SZ)
#define WS_HIDA_SZ ((size_t)MT * KS_MLP * 1024)
#define WS_HA (WS_HIDA + WS_HIDA_SZ)
#define WS_HA_SZ ((size_t)MT * KS_SSM * 1024)
#define WS_W2T (WS_HA + WS_HA_SZ)
#define WS_W2T_SZ ((size_t)NT * KS_MLP * 1024)
#define WS_CT (WS_W2T + WS_W2T_SZ)
#define WS_CT_SZ ((size_t)NT * KS_SSM * 1024)
#define WS_G (WS_CT + WS_CT_SZ)

__device__ __forceinline__ float bf2f(unsigned short u) { return __uint_as_float(((unsigned)u) << 16); }
__device__ __forceinline__ float bfr(float x) { return (float)(__bf16)x; }
__device__ __forceinline__ unsigned short f2bf(float x) {
    __bf16 b = (__bf16)x;
    return __builtin_bit_cast(unsigned short, b);
}

__device__ __forceinline__ void sincos_acc(float x, float& s, float& c) {
    float kf = rintf(x * 0.63661977236758134f);
    float r = fmaf(kf, -1.5707962513e+00f, x);
    r = fmaf(kf, -7.5497894159e-08f, r);
    r = fmaf(kf, -5.3903029534e-15f, r);
    int k = (int)kf;
    float r2 = r * r;
    float sp = fmaf(r2, -1.9515295891e-04f, 8.3321608736e-03f);
    sp = fmaf(sp, r2, -1.6666654611e-01f);
    sp = fmaf(sp * r2, r, r);
    float cp = fmaf(r2, 2.443315711809948e-05f, -1.388731625493765e-03f);
    cp = fmaf(cp, r2, 4.166664568298827e-02f);
    cp = fmaf(cp * r2, r2, fmaf(-0.5f, r2, 1.0f));
    float ss = (k & 1) ? cp : sp;
    float cc = (k & 1) ? sp : cp;
    s = (k & 2) ? -ss : ss;
    c = ((k + 1) & 2) ? -cc : cc;
}

__device__ __forceinline__ void chan_consts(float lre, float lim, float ldt, float& Lr, float& Li,
                                            float& cr, float& ci) {
    float dt = bfr(expf(ldt));
    float a = dt * lre, b = dt * lim;
    float ea = expf(a);
    float sb, cb;
    sincos_acc(b, sb, cb);
    Lr = ea * cb;
    Li = ea * sb;
    float sh, chh;
    sincos_acc(0.5f * b, sh, chh);
    float nr = expm1f(a) * cb - 2.0f * sh * sh;
    float ni = Li;
    float den = lre * lre + lim * lim;
    float inv = 1.0f / den;
    cr = (nr * lre + ni * lim) * inv;
    ci = (ni * lre - nr * lim) * inv;
}

#define CMUL_R(ar, ai, br, bi) ((ar) * (br) - (ai) * (bi))
#define CMUL_I(ar, ai, br, bi) ((ar) * (bi) + (ai) * (br))

#define K1_SSM_BLOCKS 160
#define K1_MLP_NT 14
#define K1_MLP_BLOCKS (16 * K1_MLP_NT)
#define K1_G_BLOCKS 32
#define K1_W2_BLOCKS 48
#define K1_C_BLOCKS 32
#define K1_GRID (K1_SSM_BLOCKS + K1_MLP_BLOCKS + K1_G_BLOCKS + K1_W2_BLOCKS + K1_C_BLOCKS)

__device__ __forceinline__ unsigned swz128(unsigned row, unsigned ch) {
    return row * 128u + ((ch ^ ((row >> 1) & 7u)) << 4);
}
__device__ __forceinline__ unsigned off_b(unsigned row, unsigned ch) {
    return 256u * row + 16u * (ch ^ (((row & 3u) << 2) | ((row >> 2) & 3u)));
}

struct K1Params {
    const unsigned short* x;
    const unsigned short* lre;
    const unsigned short* lim;
    const unsigned short* bre;
    const unsigned short* bim;
    const unsigned short* cre;
    const unsigned short* cim;
    const unsigned short* ldt;
    const unsigned short* w1;
    const unsigned short* w2;
    const unsigned short* gw;
    char* ws;
};

typedef __attribute__((ext_vector_type(4))) unsigned u32x4;

template <bool SSM>
__device__ __forceinline__ void k1_tile(const K1Params& p, char* smem, int mtile, int ntile) {
    const int tid = threadIdx.x;
    const int lane = tid & 63;
    const int wid = tid >> 6;
    const int t0 = mtile * 128;
    const int wt = wid >> 1, wn = wid & 1;

    const unsigned short* xsrc = p.x + (size_t)(t0 + (tid >> 3)) * DM + (tid & 7) * 8;
    const unsigned short* wsrc0;
    const unsigned short* wsrc1;
    if (SSM) {
        wsrc0 = p.bre + (size_t)(ntile * 64 + (tid >> 3)) * DM + (tid & 7) * 8;
        wsrc1 = p.bim + (size_t)(ntile * 64 + (tid >> 3)) * DM + (tid & 7) * 8;
    } else {
        int n = ntile * 128 + (tid & 15) * 8;
        if (n > FF - 8) n = FF - 8;
        wsrc0 = p.w1 + (size_t)(tid >> 4) * FF + n;
        wsrc1 = wsrc0;
    }
    unsigned xoff[4], woff[4];
#pragma unroll
    for (int it = 0; it < 4; ++it) {
        xoff[it] = swz128((tid >> 3) + 32 * it, tid & 7);
        woff[it] = SSM ? xoff[it] : off_b((tid >> 4) + 16 * it, tid & 15);
    }

    f32x16 acc[2][2];
#pragma unroll
    for (int i = 0; i < 2; ++i)
#pragma unroll
        for (int j = 0; j < 2; ++j)
#pragma unroll
            for (int r = 0; r < 16; ++r) acc[i][j][r] = 0.f;

    u32x4 xr0, xr1, xr2, xr3, wr0, wr1, wr2, wr3;
#define K1_LOAD(KT)                                                                        \
    do {                                                                                   \
        xr0 = *(const u32x4*)(xsrc + (KT) * 64);                                           \
        xr1 = *(const u32x4*)(xsrc + (KT) * 64 + 32 * DM);                                 \
        xr2 = *(const u32x4*)(xsrc + (KT) * 64 + 64 * DM);                                 \
        xr3 = *(const u32x4*)(xsrc + (KT) * 64 + 96 * DM);                                 \
        if (SSM) {                                                                         \
            wr0 = *(const u32x4*)(wsrc0 + (KT) * 64);                                      \
            wr1 = *(const u32x4*)(wsrc1 + (KT) * 64);                                      \
            wr2 = *(const u32x4*)(wsrc0 + (KT) * 64 + 32 * DM);                            \
            wr3 = *(const u32x4*)(wsrc1 + (KT) * 64 + 32 * DM);                            \
        } else {                                                                           \
            wr0 = *(const u32x4*)(wsrc0 + (size_t)(KT) * 64 * FF);                         \
            wr1 = *(const u32x4*)(wsrc0 + (size_t)((KT) * 64 + 16) * FF);                  \
            wr2 = *(const u32x4*)(wsrc0 + (size_t)((KT) * 64 + 32) * FF);                  \
            wr3 = *(const u32x4*)(wsrc0 + (size_t)((KT) * 64 + 48) * FF);                  \
        }                                                                                  \
    } while (0)
    K1_LOAD(0);

    const unsigned l31 = lane & 31, lh = lane >> 5;
    const unsigned xa0 = wt * 64 + l31, xa1 = xa0 + 32;
    const unsigned ba0 = wn * 64 + l31, ba1 = ba0 + 32;
    const unsigned tq = (lane & 15) >> 2, tp = lane & 3, tblk = (lane >> 4) & 1;

    for (int kt = 0; kt < 10; ++kt) {
        char* xs = smem + (kt & 1) * 32768;
        char* wsm = xs + 16384;
        *(u32x4*)(xs + xoff[0]) = xr0;
        *(u32x4*)(xs + xoff[1]) = xr1;
        *(u32x4*)(xs + xoff[2]) = xr2;
        *(u32x4*)(xs + xoff[3]) = xr3;
        *(u32x4*)(wsm + woff[0]) = wr0;
        *(u32x4*)(wsm + woff[1]) = wr1;
        *(u32x4*)(wsm + woff[2]) = wr2;
        *(u32x4*)(wsm + woff[3]) = wr3;
        __syncthreads();
        if (kt < 9) K1_LOAD(kt + 1);
#pragma unroll
        for (int s = 0; s < 4; ++s) {
            bf16x8 xf0 = *(const bf16x8*)(xs + swz128(xa0, 2 * s + lh));
            bf16x8 xf1 = *(const bf16x8*)(xs + swz128(xa1, 2 * s + lh));
            if (SSM) {
                bf16x8 bf0 = *(const bf16x8*)(wsm + swz128(ba0, 2 * s + lh));
                bf16x8 bf1 = *(const bf16x8*)(wsm + swz128(ba1, 2 * s + lh));
                acc[0][0] = __builtin_amdgcn_mfma_f32_32x32x16_bf16(xf0, bf0, acc[0][0], 0, 0, 0);
                acc[0][1] = __builtin_amdgcn_mfma_f32_32x32x16_bf16(xf0, bf1, acc[0][1], 0, 0, 0);
                acc[1][0] = __builtin_amdgcn_mfma_f32_32x32x16_bf16(xf1, bf0, acc[1][0], 0, 0, 0);
                acc[1][1] = __builtin_amdgcn_mfma_f32_32x32x16_bf16(xf1, bf1, acc[1][1], 0, 0, 0);
            } else {
                bf16x8 wf[2];
#pragma unroll
                for (int i = 0; i < 2; ++i) {
                    unsigned c = 2 * wn + i;
                    unsigned a0 = off_b(16 * s + 8 * lh + 0 + tq, 4 * c + 2 * tblk + (tp >> 1)) + 8 * (tp & 1);
                    unsigned a1 = off_b(16 * s + 8 * lh + 4 + tq, 4 * c + 2 * tblk + (tp >> 1)) + 8 * (tp & 1);
                    bf16x4 t0v = __builtin_amdgcn_ds_read_tr16_b64_v4bf16((lds_bf16x4_ptr)(wsm + a0));
                    bf16x4 t1v = __builtin_amdgcn_ds_read_tr16_b64_v4bf16((lds_bf16x4_ptr)(wsm + a1));
                    wf[i] = __builtin_shufflevector(t0v, t1v, 0, 1, 2, 3, 4, 5, 6, 7);
                }
                acc[0][0] = __builtin_amdgcn_mfma_f32_32x32x16_bf16(wf[0], xf0, acc[0][0], 0, 0, 0);
                acc[0][1] = __builtin_amdgcn_mfma_f32_32x32x16_bf16(wf[0], xf1, acc[0][1], 0, 0, 0);
                acc[1][0] = __builtin_amdgcn_mfma_f32_32x32x16_bf16(wf[1], xf0, acc[1][0], 0, 0, 0);
                acc[1][1] = __builtin_amdgcn_mfma_f32_32x32x16_bf16(wf[1], xf1, acc[1][1], 0, 0, 0);
            }
        }
    }
#undef K1_LOAD

    if (SSM) {
        const int ch = ntile * 64 + wn * 32 + l31;
        float Lr, Li, cr, ci;
        chan_consts(bf2f(p.lre[ch]), bf2f(p.lim[ch]), bf2f(p.ldt[ch]), Lr, Li, cr, ci);
        float L2r = CMUL_R(Lr, Li, Lr, Li), L2i = CMUL_I(Lr, Li, Lr, Li);
        float L3r = CMUL_R(L2r, L2i, Lr, Li), L3i = CMUL_I(L2r, L2i, Lr, Li);
        float L4r = CMUL_R(L2r, L2i, L2r, L2i), L4i = CMUL_I(L2r, L2i, L2r, L2i);
        f32x16 vr[2], vi[2];
#pragma unroll
        for (int mi = 0; mi < 2; ++mi)
#pragma unroll
            for (int r = 0; r < 16; ++r) {
                float ar = acc[mi][0][r], ai = acc[mi][1][r];
                vr[mi][r] = cr * ar - ci * ai;
                vi[mi][r] = cr * ai + ci * ar;
            }
#pragma unroll
        for (int mi = 0; mi < 2; ++mi)
#pragma unroll
            for (int j = 0; j < 4; ++j)
#pragma unroll
                for (int i = 1; i < 4; ++i) {
                    int b = 4 * j + i;
                    float pr = vr[mi][b - 1], pi = vi[mi][b - 1];
                    vr[mi][b] += Lr * pr - Li * pi;
                    vi[mi][b] += Lr * pi + Li * pr;
                }
        float cR = 0.f, cI = 0.f;
#pragma unroll
        for (int mi = 0; mi < 2; ++mi)
#pragma unroll
            for (int j = 0; j < 4; ++j) {
                float er = vr[mi][4 * j + 3], ei = vi[mi][4 * j + 3];
                float or_ = __shfl_xor(er, 32), oi = __shfl_xor(ei, 32);
                float E0r = lh ? or_ : er, E0i = lh ? oi : ei;
                float E1r = lh ? er : or_, E1i = lh ? ei : oi;
                float c0r = cR, c0i = cI;
                float c1r = L4r * cR - L4i * cI + E0r;
                float c1i = L4r * cI + L4i * cR + E0i;
                cR = L4r * c1r - L4i * c1i + E1r;
                cI = L4r * c1i + L4i * c1r + E1i;
                float inr = lh ? c1r : c0r, ini = lh ? c1i : c0i;
                vr[mi][4 * j + 0] += Lr * inr - Li * ini;
                vi[mi][4 * j + 0] += Lr * ini + Li * inr;
                vr[mi][4 * j + 1] += L2r * inr - L2i * ini;
                vi[mi][4 * j + 1] += L2r * ini + L2i * inr;
                vr[mi][4 * j + 2] += L3r * inr - L3i * ini;
                vi[mi][4 * j + 2] += L3r * ini + L3i * inr;
                vr[mi][4 * j + 3] += L4r * inr - L4i * ini;
                vi[mi][4 * j + 3] += L4r * ini + L4i * inr;
            }
        float2* local = (float2*)(p.ws + WS_LOCAL);
#pragma unroll
        for (int mi = 0; mi < 2; ++mi)
#pragma unroll
            for (int r = 0; r < 16; ++r) {
                int t = t0 + wt * 64 + mi * 32 + (r & 3) + 8 * (r >> 2) + 4 * lh;
                local[(size_t)t * PN + ch] = make_float2(vr[mi][r], vi[mi][r]);
            }
        if (lh == 0) {
            float2* agg = (float2*)(p.ws + WS_AGG);
            agg[(size_t)((t0 >> 6) + wt) * PN + ch] = make_float2(cR, cI);
        }
    } else {
        u32x4* hid = (u32x4*)(p.ws + WS_HIDA);
#pragma unroll
        for (int fi = 0; fi < 2; ++fi) {
            int f0 = ntile * 128 + wn * 64 + fi * 32;
            if (f0 < FF) {
                int ks = f0 >> 5;
#pragma unroll
                for (int mi = 0; mi < 2; ++mi) {
                    int t = t0 + wt * 64 + mi * 32 + l31;
                    int mt = t >> 4;
#pragma unroll
                    for (int half = 0; half < 2; ++half) {
                        u32x4 pk;
#pragma unroll
                        for (int j = 0; j < 4; ++j) {
                            float v0 = acc[fi][mi][8 * half + 2 * j];
                            float v1 = acc[fi][mi][8 * half + 2 * j + 1];
                            v0 = v0 > 0.f ? v0 : 0.f;
                            v1 = v1 > 0.f ? v1 : 0.f;
                            pk[j] = f2bf(v0) | ((unsigned)f2bf(v1) << 16);
                        }
                        int q = 2 * half + lh;
                        hid[((size_t)mt * KS_MLP + ks) * 64 + (t & 15) + 16 * q] = pk;
                    }
                }
            }
        }
    }
}

__global__ __launch_bounds__(256, 2) void k1_kernel(K1Params p) {
    __shared__ __attribute__((aligned(16))) char smem[65536];
    const int tid = threadIdx.x;
    const int lane = tid & 63;
    const int wid = tid >> 6;
    int bid = blockIdx.x;

    if (bid < K1_SSM_BLOCKS) {
        k1_tile<true>(p, smem, bid / 10, bid % 10);
        return;
    }
    if (bid < K1_SSM_BLOCKS + K1_MLP_BLOCKS) {
        int b2 = bid - K1_SSM_BLOCKS;
        k1_tile<false>(p, smem, b2 / K1_MLP_NT, b2 % K1_MLP_NT);
        return;
    }
    bid -= K1_SSM_BLOCKS + K1_MLP_BLOCKS;
    if (bid < K1_G_BLOCKS) {
        float* g = (float*)(p.ws + WS_G);
        float gwv[10];
#pragma unroll
        for (int i = 0; i < 10; ++i) gwv[i] = bf2f(p.gw[lane + 64 * i]);
        for (int r = 0; r < 16; ++r) {
            int t = (bid * 4 + wid) * 16 + r;
            float s = 0.f;
#pragma unroll
            for (int i = 0; i < 10; ++i) s += bfr(bf2f(p.x[(size_t)t * DM + lane + 64 * i]) * gwv[i]);
#pragma unroll
            for (int o = 32; o > 0; o >>= 1) s += __shfl_xor(s, o);
            if (lane == 0) {
                float z = bfr(s);
                g[t] = bfr(1.0f / (1.0f + expf(-z)));
            }
        }
        return;
    }
    bid -= K1_G_BLOCKS;
    if (bid < K1_W2_BLOCKS) {
        u32x4* w2t = (u32x4*)(p.ws + WS_W2T);
        for (int u = bid * 256 + tid; u < NT * KS_MLP * 64; u += K1_W2_BLOCKS * 256) {
            int l = u & 63, fr = u >> 6;
            int ks = fr % KS_MLP, nt = fr / KS_MLP;
            int q = l >> 4, n = nt * 16 + (l & 15);
            u32x4 pk;
#pragma unroll
            for (int j = 0; j < 4; ++j) {
                int f = 32 * ks + 16 * (q >> 1) + 8 * (j >> 1) + 4 * (q & 1) + 2 * (j & 1);
                pk[j] = p.w2[(size_t)f * DM + n] | ((unsigned)p.w2[(size_t)(f + 1) * DM + n] << 16);
            }
            w2t[u] = pk;
        }
        return;
    }
    bid -= K1_W2_BLOCKS;
    {
        u32x4* ct = (u32x4*)(p.ws + WS_CT);
        for (int u = bid * 256 + tid; u < NT * KS_SSM * 64; u += K1_C_BLOCKS * 256) {
            int l = u & 63, fr = u >> 6;
            int ks = fr % KS_SSM, nt = fr / KS_SSM;
            int q = l >> 4, d = nt * 16 + (l & 15);
            int pc = 16 * ks + 4 * q;
            uint2 re = *(const uint2*)(p.cre + (size_t)d * PN + pc);
            uint2 im = *(const uint2*)(p.cim + (size_t)d * PN + pc);
            u32x4 rr = {re.x & 0xffffu, re.x >> 16, re.y & 0xffffu, re.y >> 16};
            u32x4 ii = {im.x & 0xffffu, im.x >> 16, im.y & 0xffffu, im.y >> 16};
            u32x4 pk;
#pragma unroll
            for (int c = 0; c < 4; ++c) {
                unsigned short a = f2bf(2.0f * bf2f((unsigned short)rr[c]));
                unsigned short b = f2bf(-2.0f * bf2f((unsigned short)ii[c]));
                pk[c] = a | ((unsigned)b << 16);
            }
            ct[u] = pk;
        }
    }
}

template <int KS>
__device__ __forceinline__ void gemm2_body(const uint4* __restrict__ A, const uint4* __restrict__ B,
                                           int mb, int nb, float* red, f32x4 (&out)[5]) {
    const int lane = threadIdx.x & 63, wid = threadIdx.x >> 6;
    f32x4 acc[4][5];
#pragma unroll
    for (int i = 0; i < 4; ++i)
#pragma unroll
        for (int j = 0; j < 5; ++j) acc[i][j] = (f32x4){0.f, 0.f, 0.f, 0.f};
    const uint4* ap = A + ((size_t)(mb * 4) * KS) * 64 + lane;
    const uint4* bp = B + ((size_t)(nb * 5) * KS) * 64 + lane;
    uint4 af[4], bfr_[5];
    int ks = wid;
#pragma unroll
    for (int i = 0; i < 4; ++i) af[i] = ap[((size_t)i * KS + ks) * 64];
#pragma unroll
    for (int j = 0; j < 5; ++j) bfr_[j] = bp[((size_t)j * KS + ks) * 64];
    for (; ks < KS; ks += 4) {
        uint4 an[4], bn[5];
        int kn = ks + 4;
        if (kn < KS) {
#pragma unroll
            for (int i = 0; i < 4; ++i) an[i] = ap[((size_t)i * KS + kn) * 64];
#pragma unroll
            for (int j = 0; j < 5; ++j) bn[j] = bp[((size_t)j * KS + kn) * 64];
        }
#pragma unroll
        for (int i = 0; i < 4; ++i)
#pragma unroll
            for (int j = 0; j < 5; ++j)
                acc[i][j] = __builtin_amdgcn_mfma_f32_16x16x32_bf16(__builtin_bit_cast(bf16x8, af[i]),
                                                                    __builtin_bit_cast(bf16x8, bfr_[j]),
                                                                    acc[i][j], 0, 0, 0);
        if (kn < KS) {
#pragma unroll
            for (int i = 0; i < 4; ++i) af[i] = an[i];
#pragma unroll
            for (int j = 0; j < 5; ++j) bfr_[j] = bn[j];
        }
    }
    f32x4* r4 = (f32x4*)red;
#pragma unroll
    for (int i = 0; i < 4; ++i)
        if (i != wid) {
#pragma unroll
            for (int j = 0; j < 5; ++j) r4[((wid * 4 + i) * 5 + j) * 64 + lane] = acc[i][j];
        }
    __syncthreads();
#pragma unroll
    for (int j = 0; j < 5; ++j) {
        f32x4 s = (f32x4){0.f, 0.f, 0.f, 0.f};
#pragma unroll
        for (int w = 0; w < 4; ++w) {
            f32x4 v;
            if (w == wid) {
                v = acc[0][j];
#pragma unroll
                for (int i = 1; i < 4; ++i)
                    if (i == wid) v = acc[i][j];
            } else {
                v = r4[((w * 4 + wid) * 5 + j) * 64 + lane];
            }
            s += v;
        }
        out[j] = s;
    }
}

#define K2_GEMM_BLOCKS 256
#define K2_FIX_BLOCKS (NCHUNK * 10)
struct K2Params {
    const unsigned short* lre;
    const unsigned short* lim;
    const unsigned short* ldt;
    char* ws;
    float* out;
};

__global__ __launch_bounds__(256, 2) void k2_kernel(K2Params p) {
    __shared__ __attribute__((aligned(16))) float smem[4 * 4 * 5 * 64 * 4];
    const int tid = threadIdx.x, lane = tid & 63, wid = tid >> 6;
    int bid = blockIdx.x;
    if (bid < K2_GEMM_BLOCKS) {
        int xcd = bid & 7, idx = bid >> 3;
        int mb = xcd * 4 + (idx >> 3), nb = idx & 7;
        f32x4 o[5];
        gemm2_body<KS_MLP>((const uint4*)(p.ws + WS_HIDA), (const uint4*)(p.ws + WS_W2T), mb, nb, smem, o);
        const float* g = (const float*)(p.ws + WS_G);
        int trow = mb * 64 + wid * 16 + (lane >> 4) * 4;
#pragma unroll
        for (int r = 0; r < 4; ++r) {
            float omg = bfr(1.0f - g[trow + r]);
#pragma unroll
            for (int j = 0; j < 5; ++j) {
                int d = nb * 80 + j * 16 + (lane & 15);
                p.out[(size_t)(trow + r) * DM + d] = bfr(omg * bfr(o[j][r]));
            }
        }
        return;
    }
    bid -= K2_GEMM_BLOCKS;
    {
        const int c = bid / 10, cg = bid % 10;
        float2* corr = (float2*)smem;
        if (tid < 64) {
            int ch = cg * 64 + tid;
            float Lr, Li, cr, ci;
            chan_consts(bf2f(p.lre[ch]), bf2f(p.lim[ch]), bf2f(p.ldt[ch]), Lr, Li, cr, ci);
            float Pr = Lr, Pi = Li;
#pragma unroll
            for (int i = 0; i < 6; ++i) {
                float nr = Pr * Pr - Pi * Pi, ni = 2.f * Pr * Pi;
                Pr = nr; Pi = ni;
            }
            const float2* agg = (const float2*)(p.ws + WS_AGG);
            float Hr = 0.f, Hi = 0.f;
            for (int cc = 0; cc < c; ++cc) {
                float2 e = agg[(size_t)cc * PN + ch];
                float nr = Pr * Hr - Pi * Hi + e.x;
                float ni = Pr * Hi + Pi * Hr + e.y;
                Hr = nr; Hi = ni;
            }
            for (int n = 0; n < 64; ++n) {
                float nr = Lr * Hr - Li * Hi, ni = Lr * Hi + Li * Hr;
                Hr = nr; Hi = ni;
                corr[n * 64 + tid] = make_float2(Hr, Hi);
            }
        }
        __syncthreads();
        const float4* local = (const float4*)(p.ws + WS_LOCAL);
        uint4* ha = (uint4*)(p.ws + WS_HA);
        const int tl = wid * 16 + (lane & 15);
        const int q = lane >> 4;
        const int t = c * 64 + tl;
#pragma unroll
        for (int k4 = 0; k4 < 4; ++k4) {
            int chl = k4 * 16 + 4 * q;
            int ch = cg * 64 + chl;
            float4 v0 = local[((size_t)t * PN + ch) / 2];
            float4 v1 = local[((size_t)t * PN + ch) / 2 + 1];
            float4 c0 = *(const float4*)&corr[tl * 64 + chl];
            float4 c1 = *(const float4*)&corr[tl * 64 + chl + 2];
            uint4 pk;
            pk.x = f2bf(v0.x + c0.x) | ((unsigned)f2bf(v0.y + c0.y) << 16);
            pk.y = f2bf(v0.z + c0.z) | ((unsigned)f2bf(v0.w + c0.w) << 16);
            pk.z = f2bf(v1.x + c1.x) | ((unsigned)f2bf(v1.y + c1.y) << 16);
            pk.w = f2bf(v1.z + c1.z) | ((unsigned)f2bf(v1.w + c1.w) << 16);
            int ks = cg * 4 + k4, mt = t >> 4;
            ha[((size_t)mt * KS_SSM + ks) * 64 + lane] = pk;
        }
    }
}

struct K3Params {
    const unsigned short* x;
    const unsigned short* dskip;
    char* ws;
    float* out;
};

__global__ __launch_bounds__(256, 2) void k3_kernel(K3Params p) {
    __shared__ __attribute__((aligned(16))) float smem[4 * 4 * 5 * 64 * 4];
    const int tid = threadIdx.x, lane = tid & 63, wid = tid >> 6;
    int bid = blockIdx.x;
    int xcd = bid & 7, idx = bid >> 3;
    int mb = xcd * 4 + (idx >> 3), nb = idx & 7;
    f32x4 o[5];
    gemm2_body<KS_SSM>((const uint4*)(p.ws + WS_HA), (const uint4*)(p.ws + WS_CT), mb, nb, smem, o);
    const float* g = (const float*)(p.ws + WS_G);
    int trow = mb * 64 + wid * 16 + (lane >> 4) * 4;
#pragma unroll
    for (int r = 0; r < 4; ++r) {
        float gg = g[trow + r];
#pragma unroll
        for (int j = 0; j < 5; ++j) {
            int d = nb * 80 + j * 16 + (lane & 15);
            size_t idx2 = (size_t)(trow + r) * DM + d;
            float skip = bfr(bf2f(p.x[idx2]) * bf2f(p.dskip[d]));
            p.out[idx2] = gg * (o[j][r] + skip) + p.out[idx2];
        }
    }
}

extern "C" void kernel_launch(void* const* d_in, const int* in_sizes, int n_in, void* d_out, int out_size,
                              void* d_ws, size_t ws_size, hipStream_t stream) {
    const unsigned short* x = (const unsigned short*)d_in[0];
    const unsigned short* lre = (const unsigned short*)d_in[1];
    const unsigned short* lim = (const unsigned short*)d_in[2];
    const unsigned short* bre = (const unsigned short*)d_in[3];
    const unsigned short* bim = (const unsigned short*)d_in[4];
    const unsigned short* cre = (const unsigned short*)d_in[5];
    const unsigned short* cim = (const unsigned short*)d_in[6];
    const unsigned short* ldt = (const unsigned short*)d_in[7];
    const unsigned short* dskip = (const unsigned short*)d_in[8];
    const unsigned short* w1 = (const unsigned short*)d_in[10];
    const unsigned short* w2 = (const unsigned short*)d_in[11];
    const unsigned short* gw = (const unsigned short*)d_in[12];
    char* ws = (char*)d_ws;
    float* out = (float*)d_out;

    K1Params p1{x, lre, lim, bre, bim, cre, cim, ldt, w1, w2, gw, ws};
    k1_kernel<<<K1_GRID, 256, 0, stream>>>(p1);
    K2Params p2{lre, lim, ldt, ws, out};
    k2_kernel<<<K2_GEMM_BLOCKS + K2_FIX_BLOCKS, 256, 0, stream>>>(p2);
    K3Params p3{x, dskip, ws, out};
    k3_kernel<<<256, 256, 0, stream>>>(p3);
}
```

```cpp
#include <hip/hip_runtime.h>
#include <hip/hip_bf16.h>
#include <stdint.h>

typedef __attribute__((ext_vector_type(8))) __bf16 bf16x8;
typedef __attribute__((ext_vector_type(4))) __bf16 bf16x4;
typedef __attribute__((ext_vector_type(16))) float f32x16;
typedef __attribute__((ext_vector_type(4))) float f32x4;
typedef __attribute__((ext_vector_type(2))) float f32x2;
typedef __attribute__((ext_vector_type(4))) unsigned u32x4;
typedef __attribute__((ext_vector_type(2))) unsigned u32x2;
typedef __attribute__((address_space(3))) bf16x4* lds_bf16x4_ptr;

#define LSEQ 2048
#define DM 640
#define PN 640
#define FF 1728
#define CHUNK 64
#define NCHUNK (LSEQ / CHUNK)
#define KS_MLP (FF / 32)
#define KS_SSM (2 * PN / 32)
#define MT (LSEQ / 16)
#define NT (DM / 16)

#define WS_LOCAL 0
#define WS_LOCAL_SZ ((size_t)LSEQ * PN * 8)
#define WS_AGG (WS_LOCAL + WS_LOCAL_SZ)
#define WS_AGG_SZ ((size_t)NCHUNK * PN * 8)
#define WS_HIDA (WS_AGG + WS_AGG_SZ)
#define WS_HIDA_SZ ((size_t)MT * KS_MLP * 1024)
#define WS_HA (WS_HIDA + WS_HIDA_SZ)
#define WS_HA_SZ ((size_t)MT * KS_SSM * 1024)
#define WS_W2T (WS_HA + WS_HA_SZ)
#define WS_W2T_SZ ((size_t)NT * KS_MLP * 1024)
#define WS_CT (WS_W2T + WS_W2T_SZ)
#define WS_CT_SZ ((size_t)NT * KS_SSM * 1024)
#define WS_G (WS_CT + WS_CT_SZ)

#define STAMP(kid, idx) do {} while (0)
#define STAMP_DRAIN(kid, idx) do {} while (0)

__device__ __forceinline__ float bf2f(unsigned short u) { return __uint_as_float(((unsigned)u) << 16); }
__device__ __forceinline__ float bflo(unsigned u) { return __uint_as_float(u << 16); }
__device__ __forceinline__ float bfhi(unsigned u) { return __uint_as_float(u & 0xffff0000u); }
__device__ __forceinline__ float bfr(float x) { return (float)(__bf16)x; }
__device__ __forceinline__ unsigned short f2bf(float x) {
    __bf16 b = (__bf16)x;
    return __builtin_bit_cast(unsigned short, b);
}
__device__ __forceinline__ unsigned pk2(float a, float b) { return f2bf(a) | ((unsigned)f2bf(b) << 16); }

__device__ __forceinline__ void sincos_acc(float x, float& s, float& c) {
    float kf = rintf(x * 0.63661977236758134f);
    float r = fmaf(kf, -1.5707962513e+00f, x);
    r = fmaf(kf, -7.5497894159e-08f, r);
    r = fmaf(kf, -5.3903029534e-15f, r);
    int k = (int)kf;
    float r2 = r * r;
    float sp = fmaf(r2, -1.9515295891e-04f, 8.3321608736e-03f);
    sp = fmaf(sp, r2, -1.6666654611e-01f);
    sp = fmaf(sp * r2, r, r);
    float cp = fmaf(r2, 2.443315711809948e-05f, -1.388731625493765e-03f);
    cp = fmaf(cp, r2, 4.166664568298827e-02f);
    cp = fmaf(cp * r2, r2, fmaf(-0.5f, r2, 1.0f));
    float ss = (k & 1) ? cp : sp;
    float cc = (k & 1) ? sp : cp;
    s = (k & 2) ? -ss : ss;
    c = ((k + 1) & 2) ? -cc : cc;
}

__device__ __forceinline__ void chan_lam(float lre, float lim, float ldt, float& Lr, float& Li) {
    float dt = bfr(expf(ldt));
    float a = dt * lre, b = dt * lim;
    float ea = expf(a);
    float sb, cb;
    sincos_acc(b, sb, cb);
    Lr = ea * cb;
    Li = ea * sb;
}
__device__ __forceinline__ void chan_consts(float lre, float lim, float ldt, float& Lr, float& Li,
                                            float& cr, float& ci) {
    float dt = bfr(expf(ldt));
    float a = dt * lre, b = dt * lim;
    float ea = expf(a);
    float sb, cb;
    sincos_acc(b, sb, cb);
    Lr = ea * cb;
    Li = ea * sb;
    float sh, chh;
    sincos_acc(0.5f * b, sh, chh);
    float nr = expm1f(a) * cb - 2.0f * sh * sh;
    float ni = Li;
    float den = lre * lre + lim * lim;
    float inv = 1.0f / den;
    cr = (nr * lre + ni * lim) * inv;
    ci = (ni * lre - nr * lim) * inv;
}

#define CMUL_R(ar, ai, br, bi) ((ar) * (br) - (ai) * (bi))
#define CMUL_I(ar, ai, br, bi) ((ar) * (bi) + (ai) * (br))

#define K1_GEMM_BLOCKS 384
#define K1_G_BLOCKS 128
#define K1_W2_BLOCKS 135
#define K1_C_BLOCKS 100
#define K1_GRID (K1_GEMM_BLOCKS + K1_G_BLOCKS + K1_W2_BLOCKS + K1_C_BLOCKS)

__device__ __forceinline__ unsigned swz128(unsigned row, unsigned ch) {
    return row * 128u + ((ch ^ ((row >> 1) & 7u)) << 4);
}
__device__ __forceinline__ unsigned off_b(unsigned row, unsigned ch) {
    return 256u * row + 16u * (ch ^ (((row & 3u) << 2) | ((row >> 2) & 3u)));
}

struct K1Params {
    const unsigned short* x;
    const unsigned short* lre;
    const unsigned short* lim;
    const unsigned short* bre;
    const unsigned short* bim;
    const unsigned short* cre;
    const unsigned short* cim;
    const unsigned short* ldt;
    const unsigned short* w1;
    const unsigned short* w2;
    const unsigned short* gw;
    char* ws;
};

template <bool SSM>
__device__ __forceinline__ void k1_tile(const K1Params& p, char* smem, int mtile, int ntile) {
    const int tid = threadIdx.x;
    const int lane = tid & 63;
    const int wid = tid >> 6;
    const int t0 = mtile * 128;
    const int wt = wid >> 1, wn = wid & 1;
    char* stamp_base = p.ws;
    STAMP(0, 0);

    const unsigned short* xsrc = p.x + (size_t)(t0 + (tid >> 3)) * DM + (tid & 7) * 8;
    const unsigned short* wsrc0;
    const unsigned short* wsrc1;
    if (SSM) {
        wsrc0 = p.bre + (size_t)(ntile * 64 + (tid >> 3)) * DM + (tid & 7) * 8;
        wsrc1 = p.bim + (size_t)(ntile * 64 + (tid >> 3)) * DM + (tid & 7) * 8;
    } else {
        int n = ntile * 128 + (tid & 15) * 8;
        if (n > FF - 8) n = FF - 8;
        wsrc0 = p.w1 + (size_t)(tid >> 4) * FF + n;
        wsrc1 = wsrc0;
    }
    unsigned xoff[4], woff[4];
#pragma unroll
    for (int it = 0; it < 4; ++it) {
        xoff[it] = swz128((tid >> 3) + 32 * it, tid & 7);
        woff[it] = SSM ? xoff[it] : off_b((tid >> 4) + 16 * it, tid & 15);
    }

    f32x16 acc[2][2];
#pragma unroll
    for (int i = 0; i < 2; ++i)
#pragma unroll
        for (int j = 0; j < 2; ++j)
#pragma unroll
            for (int r = 0; r < 16; ++r) acc[i][j][r] = 0.f;

    const unsigned l31 = lane & 31, lh = lane >> 5;
    const int ch = ntile * 64 + wn * 32 + l31;
    unsigned short c_lre = 0, c_lim = 0, c_ldt = 0;
    if (SSM) { c_lre = p.lre[ch]; c_lim = p.lim[ch]; c_ldt = p.ldt[ch]; }

    u32x4 xrA[4], wrA[4], xrB[4], wrB[4], xrC[4], wrC[4];
#define K1_LOAD(XR, WR, KT)                                                                \
    do {                                                                                   \
        XR[0] = *(const u32x4*)(xsrc + (KT) * 64);                                         \
        XR[1] = *(const u32x4*)(xsrc + (KT) * 64 + 32 * DM);                               \
        XR[2] = *(const u32x4*)(xsrc + (KT) * 64 + 64 * DM);                               \
        XR[3] = *(const u32x4*)(xsrc + (KT) * 64 + 96 * DM);                               \
        if (SSM) {                                                                         \
            WR[0] = *(const u32x4*)(wsrc0 + (KT) * 64);                                    \
            WR[1] = *(const u32x4*)(wsrc1 + (KT) * 64);                                    \
            WR[2] = *(const u32x4*)(wsrc0 + (KT) * 64 + 32 * DM);                          \
            WR[3] = *(const u32x4*)(wsrc1 + (KT) * 64 + 32 * DM);                          \
        } else {                                                                           \
            WR[0] = *(const u32x4*)(wsrc0 + (size_t)(KT) * 64 * FF);                       \
            WR[1] = *(const u32x4*)(wsrc0 + (size_t)((KT) * 64 + 16) * FF);                \
            WR[2] = *(const u32x4*)(wsrc0 + (size_t)((KT) * 64 + 32) * FF);                \
            WR[3] = *(const u32x4*)(wsrc0 + (size_t)((KT) * 64 + 48) * FF);                \
        }                                                                                  \
    } while (0)
    K1_LOAD(xrA, wrA, 0);
    K1_LOAD(xrB, wrB, 1);
    K1_LOAD(xrC, wrC, 2);
    __builtin_amdgcn_sched_barrier(0);

    float Lr = 0.f, Li = 0.f, cr = 0.f, ci = 0.f;
    if (SSM) chan_consts(bf2f(c_lre), bf2f(c_lim), bf2f(c_ldt), Lr, Li, cr, ci);
    __builtin_amdgcn_sched_barrier(0);

    const unsigned xa0 = wt * 64 + l31, xa1 = xa0 + 32;
    const unsigned ba0 = wn * 64 + l31, ba1 = ba0 + 32;
    const unsigned tq = (lane & 15) >> 2, tp = lane & 3, tblk = (lane >> 4) & 1;

#define K1_STEP(XR, WR, KT)                                                                          \
    do {                                                                                             \
        char* xs = smem + ((KT) & 1) * 32768;                                                        \
        char* wsm = xs + 16384;                                                                      \
        _Pragma("unroll") for (int it = 0; it < 4; ++it) {                                           \
            *(u32x4*)(xs + xoff[it]) = XR[it];                                                       \
            *(u32x4*)(wsm + woff[it]) = WR[it];                                                      \
        }                                                                                            \
        __syncthreads();                                                                             \
        if ((KT) + 3 < 10) K1_LOAD(XR, WR, (KT) + 3);                                                \
        __builtin_amdgcn_sched_barrier(0);                                                           \
        _Pragma("unroll") for (int s = 0; s < 4; ++s) {                                              \
            bf16x8 xf0 = *(const bf16x8*)(xs + swz128(xa0, 2 * s + lh));                             \
            bf16x8 xf1 = *(const bf16x8*)(xs + swz128(xa1, 2 * s + lh));                             \
            if (SSM) {                                                                               \
                bf16x8 bf0 = *(const bf16x8*)(wsm + swz128(ba0, 2 * s + lh));                        \
                bf16x8 bf1 = *(const bf16x8*)(wsm + swz128(ba1, 2 * s + lh));                        \
                acc[0][0] = __builtin_amdgcn_mfma_f32_32x32x16_bf16(xf0, bf0, acc[0][0], 0, 0, 0);   \
                acc[0][1] = __builtin_amdgcn_mfma_f32_32x32x16_bf16(xf0, bf1, acc[0][1], 0, 0, 0);   \
                acc[1][0] = __builtin_amdgcn_mfma_f32_32x32x16_bf16(xf1, bf0, acc[1][0], 0, 0, 0);   \
                acc[1][1] = __builtin_amdgcn_mfma_f32_32x32x16_bf16(xf1, bf1, acc[1][1], 0, 0, 0);   \
            } else {                                                                                 \
                bf16x8 wf[2];                                                                        \
                _Pragma("unroll") for (int i = 0; i < 2; ++i) {                                      \
                    unsigned c = 2 * wn + i;                                                         \
                    unsigned a0 = off_b(16 * s + 8 * lh + 0 + tq, 4 * c + 2 * tblk + (tp >> 1)) + 8 * (tp & 1); \
                    unsigned a1 = off_b(16 * s + 8 * lh + 4 + tq, 4 * c + 2 * tblk + (tp >> 1)) + 8 * (tp & 1); \
                    bf16x4 t0v = __builtin_amdgcn_ds_read_tr16_b64_v4bf16((lds_bf16x4_ptr)(wsm + a0)); \
                    bf16x4 t1v = __builtin_amdgcn_ds_read_tr16_b64_v4bf16((lds_bf16x4_ptr)(wsm + a1)); \
                    wf[i] = __builtin_shufflevector(t0v, t1v, 0, 1, 2, 3, 4, 5, 6, 7);               \
                }                                                                                    \
                acc[0][0] = __builtin_amdgcn_mfma_f32_32x32x16_bf16(wf[0], xf0, acc[0][0], 0, 0, 0); \
                acc[0][1] = __builtin_amdgcn_mfma_f32_32x32x16_bf16(wf[0], xf1, acc[0][1], 0, 0, 0); \
                acc[1][0] = __builtin_amdgcn_mfma_f32_32x32x16_bf16(wf[1], xf0, acc[1][0], 0, 0, 0); \
                acc[1][1] = __builtin_amdgcn_mfma_f32_32x32x16_bf16(wf[1], xf1, acc[1][1], 0, 0, 0); \
            }                                                                                        \
        }                                                                                            \
    } while (0)
    K1_STEP(xrA, wrA, 0);
    K1_STEP(xrB, wrB, 1);
    K1_STEP(xrC, wrC, 2);
    K1_STEP(xrA, wrA, 3);
    K1_STEP(xrB, wrB, 4);
    K1_STEP(xrC, wrC, 5);
    K1_STEP(xrA, wrA, 6);
    K1_STEP(xrB, wrB, 7);
    K1_STEP(xrC, wrC, 8);
    K1_STEP(xrA, wrA, 9);
#undef K1_STEP
#undef K1_LOAD
    STAMP(0, SSM ? 1 : 2);

    if (SSM) {
        float L2r = CMUL_R(Lr, Li, Lr, Li), L2i = CMUL_I(Lr, Li, Lr, Li);
        float L3r = CMUL_R(L2r, L2i, Lr, Li), L3i = CMUL_I(L2r, L2i, Lr, Li);
        float L4r = CMUL_R(L2r, L2i, L2r, L2i), L4i = CMUL_I(L2r, L2i, L2r, L2i);
        f32x16 vr[2], vi[2];
#pragma unroll
        for (int mi = 0; mi < 2; ++mi)
#pragma unroll
            for (int r = 0; r < 16; ++r) {
                float ar = acc[mi][0][r], ai = acc[mi][1][r];
                vr[mi][r] = cr * ar - ci * ai;
                vi[mi][r] = cr * ai + ci * ar;
            }
#pragma unroll
        for (int mi = 0; mi < 2; ++mi)
#pragma unroll
            for (int j = 0; j < 4; ++j)
#pragma unroll
                for (int i = 1; i < 4; ++i) {
                    int b = 4 * j + i;
                    float pr = vr[mi][b - 1], pi = vi[mi][b - 1];
                    vr[mi][b] += Lr * pr - Li * pi;
                    vi[mi][b] += Lr * pi + Li * pr;
                }
        float cR = 0.f, cI = 0.f;
#pragma unroll
        for (int mi = 0; mi < 2; ++mi)
#pragma unroll
            for (int j = 0; j < 4; ++j) {
                unsigned er = __float_as_uint(vr[mi][4 * j + 3]), ei = __float_as_uint(vi[mi][4 * j + 3]);
                auto sr = __builtin_amdgcn_permlane32_swap(er, er, false, false);
                auto si = __builtin_amdgcn_permlane32_swap(ei, ei, false, false);
                float E0r = __uint_as_float(sr[0]), E0i = __uint_as_float(si[0]);
                float E1r = __uint_as_float(sr[1]), E1i = __uint_as_float(si[1]);
                float c0r = cR, c0i = cI;
                float c1r = L4r * cR - L4i * cI + E0r;
                float c1i = L4r * cI + L4i * cR + E0i;
                cR = L4r * c1r - L4i * c1i + E1r;
                cI = L4r * c1i + L4i * c1r + E1i;
                float inr = lh ? c1r : c0r, ini = lh ? c1i : c0i;
                vr[mi][4 * j + 0] += Lr * inr - Li * ini;
                vi[mi][4 * j + 0] += Lr * ini + Li * inr;
                vr[mi][4 * j + 1] += L2r * inr - L2i * ini;
                vi[mi][4 * j + 1] += L2r * ini + L2i * inr;
                vr[mi][4 * j + 2] += L3r * inr - L3i * ini;
                vi[mi][4 * j + 2] += L3r * ini + L3i * inr;
                vr[mi][4 * j + 3] += L4r * inr - L4i * ini;
                vi[mi][4 * j + 3] += L4r * ini + L4i * inr;
            }
        f32x2* local = (f32x2*)(p.ws + WS_LOCAL);
#pragma unroll
        for (int mi = 0; mi < 2; ++mi)
#pragma unroll
            for (int r = 0; r < 16; ++r) {
                int t = t0 + wt * 64 + mi * 32 + (r & 3) + 8 * (r >> 2) + 4 * lh;
                local[(size_t)t * PN + ch] = (f32x2){vr[mi][r], vi[mi][r]};
            }
        if (lh == 0) {
            f32x2* agg = (f32x2*)(p.ws + WS_AGG);
            agg[(size_t)((t0 >> 6) + wt) * PN + ch] = (f32x2){cR, cI};
        }
        STAMP(0, 3);
        STAMP_DRAIN(0, 4);
    } else {
        u32x4* hid = (u32x4*)(p.ws + WS_HIDA);
#pragma unroll
        for (int fi = 0; fi < 2; ++fi) {
            int f0 = ntile * 128 + wn * 64 + fi * 32;
            if (f0 < FF) {
                int ks = f0 >> 5;
#pragma unroll
                for (int mi = 0; mi < 2; ++mi) {
                    int t = t0 + wt * 64 + mi * 32 + l31;
                    int mt = t >> 4;
#pragma unroll
                    for (int half = 0; half < 2; ++half) {
                        u32x4 pk;
#pragma unroll
                        for (int j = 0; j < 4; ++j) {
                            float v0 = acc[fi][mi][8 * half + 2 * j];
                            float v1 = acc[fi][mi][8 * half + 2 * j + 1];
                            v0 = v0 > 0.f ? v0 : 0.f;
                            v1 = v1 > 0.f ? v1 : 0.f;
                            pk[j] = pk2(v0, v1);
                        }
                        int q = 2 * half + lh;
                        hid[((size_t)mt * KS_MLP + ks) * 64 + (t & 15) + 16 * q] = pk;
                    }
                }
            }
        }
        STAMP_DRAIN(0, 5);
    }
}

__global__ __launch_bounds__(256, 2) void k1_kernel(K1Params p) {
    __shared__ __attribute__((aligned(16))) char smem[65536];
    const int tid = threadIdx.x;
    const int lane = tid & 63;
    const int wid = tid >> 6;
    int bid = blockIdx.x;

    if (bid < K1_GEMM_BLOCKS) {
        int xcd = bid & 7, idx = bid >> 3;
        int nt24 = xcd * 3 + idx / 16, mtile = idx % 16;
        if (nt24 < 10) k1_tile<true>(p, smem, mtile, nt24);
        else k1_tile<false>(p, smem, mtile, nt24 - 10);
        return;
    }
    bid -= K1_GEMM_BLOCKS;
    if (bid < K1_G_BLOCKS) {
        float* g = (float*)(p.ws + WS_G);
        const int t = (bid * 4 + wid) * 4 + (lane >> 4);
        const int l16 = lane & 15;
        u32x4 xv[5], gv[5];
#pragma unroll
        for (int i = 0; i < 5; ++i) {
            xv[i] = *(const u32x4*)(p.x + (size_t)t * DM + (l16 + 16 * i) * 8);
            gv[i] = *(const u32x4*)(p.gw + (l16 + 16 * i) * 8);
        }
        float sum = 0.f;
#pragma unroll
        for (int i = 0; i < 5; ++i)
#pragma unroll
            for (int e = 0; e < 4; ++e) {
                sum += bfr(bflo(xv[i][e]) * bflo(gv[i][e]));
                sum += bfr(bfhi(xv[i][e]) * bfhi(gv[i][e]));
            }
#pragma unroll
        for (int o = 8; o > 0; o >>= 1) sum += __shfl_xor(sum, o);
        if (l16 == 0) {
            float z = bfr(sum);
            g[t] = bfr(1.0f / (1.0f + expf(-z)));
        }
        return;
    }
    bid -= K1_G_BLOCKS;
    if (bid < K1_W2_BLOCKS) {
        u32x4* w2t = (u32x4*)(p.ws + WS_W2T);
        u32x4 pk[4];
#pragma unroll
        for (int i = 0; i < 4; ++i) {
            int u = (bid * 4 + i) * 256 + tid;
            int l = u & 63, fr = u >> 6;
            int ks = fr % KS_MLP, nt = fr / KS_MLP;
            int q = l >> 4, n = nt * 16 + (l & 15);
#pragma unroll
            for (int j = 0; j < 4; ++j) {
                int f = 32 * ks + 16 * (q >> 1) + 8 * (j >> 1) + 4 * (q & 1) + 2 * (j & 1);
                pk[i][j] = p.w2[(size_t)f * DM + n] | ((unsigned)p.w2[(size_t)(f + 1) * DM + n] << 16);
            }
        }
#pragma unroll
        for (int i = 0; i < 4; ++i) w2t[(bid * 4 + i) * 256 + tid] = pk[i];
        return;
    }
    bid -= K1_W2_BLOCKS;
    {
        u32x4* ct = (u32x4*)(p.ws + WS_CT);
        u32x2 re[4], im[4];
#pragma unroll
        for (int i = 0; i < 4; ++i) {
            int u = (bid * 4 + i) * 256 + tid;
            int l = u & 63, fr = u >> 6;
            int ks = fr % KS_SSM, nt = fr / KS_SSM;
            int q = l >> 4, d = nt * 16 + (l & 15);
            int pc = 16 * ks + 4 * q;
            re[i] = *(const u32x2*)(p.cre + (size_t)d * PN + pc);
            im[i] = *(const u32x2*)(p.cim + (size_t)d * PN + pc);
        }
#pragma unroll
        for (int i = 0; i < 4; ++i) {
            u32x4 pk;
            pk[0] = pk2(2.0f * bflo(re[i][0]), -2.0f * bflo(im[i][0]));
            pk[1] = pk2(2.0f * bfhi(re[i][0]), -2.0f * bfhi(im[i][0]));
            pk[2] = pk2(2.0f * bflo(re[i][1]), -2.0f * bflo(im[i][1]));
            pk[3] = pk2(2.0f * bfhi(re[i][1]), -2.0f * bfhi(im[i][1]));
            ct[(bid * 4 + i) * 256 + tid] = pk;
        }
    }
}

struct Frag9 { u32x4 a[4]; u32x4 b[5]; };

template <int KS>
__device__ __forceinline__ void g2_load(Frag9& f, const u32x4* __restrict__ ap, const u32x4* __restrict__ bp, int ks) {
#pragma unroll
    for (int i = 0; i < 4; ++i) f.a[i] = ap[((size_t)i * KS + ks) * 64];
#pragma unroll
    for (int j = 0; j < 5; ++j) f.b[j] = bp[((size_t)j * KS + ks) * 64];
    __builtin_amdgcn_sched_barrier(0);
}
__device__ __forceinline__ void g2_mma(const Frag9& f, f32x4 (&acc)[4][5]) {
#pragma unroll
    for (int i = 0; i < 4; ++i)
#pragma unroll
        for (int j = 0; j < 5; ++j)
            acc[i][j] = __builtin_amdgcn_mfma_f32_16x16x32_bf16(__builtin_bit_cast(bf16x8, f.b[j]),
                                                                __builtin_bit_cast(bf16x8, f.a[i]), acc[i][j], 0, 0, 0);
    __builtin_amdgcn_sched_barrier(0);
}

template <int KS, int NW, int MODE>
__device__ __forceinline__ void gemm2_body(const u32x4* __restrict__ A, const u32x4* __restrict__ B,
                                           int mb, int nb, float* red, const float* __restrict__ g,
                                           const unsigned short* __restrict__ x,
                                           const unsigned short* __restrict__ dskip, float* __restrict__ out,
                                           char* stamp_base, int kid) {
    STAMP(kid, 0);
    const int lane = threadIdx.x & 63;
    const int wid = __builtin_amdgcn_readfirstlane(threadIdx.x >> 6);
    constexpr int NS = (KS + NW - 1) / NW;
    f32x4 acc[4][5];
#pragma unroll
    for (int i = 0; i < 4; ++i)
#pragma unroll
        for (int j = 0; j < 5; ++j) acc[i][j] = (f32x4){0.f, 0.f, 0.f, 0.f};
    const u32x4* ap = A + ((size_t)(mb * 4) * KS) * 64 + lane;
    const u32x4* bp = B + ((size_t)(nb * 5) * KS) * 64 + lane;
    Frag9 f0, f1, f2, f3;
    g2_load<KS>(f0, ap, bp, wid);
    g2_load<KS>(f1, ap, bp, wid + NW);
    g2_load<KS>(f2, ap, bp, wid + 2 * NW);
    g2_load<KS>(f3, ap, bp, wid + 3 * NW);
    STAMP(kid, 1);
#pragma unroll
    for (int s = 0; s < NS; ++s) {
        const int kn = wid + NW * (s + 4);
        const bool last_ragged = (s == NS - 1) && (KS % NW != 0);
        const bool do_mma = !last_ragged || (wid + NW * s < KS);
        if ((s & 3) == 0) { if (do_mma) g2_mma(f0, acc); if (s + 4 < NS) g2_load<KS>(f0, ap, bp, kn < KS ? kn : KS - 1); }
        if ((s & 3) == 1) { if (do_mma) g2_mma(f1, acc); if (s + 4 < NS) g2_load<KS>(f1, ap, bp, kn < KS ? kn : KS - 1); }
        if ((s & 3) == 2) { if (do_mma) g2_mma(f2, acc); if (s + 4 < NS) g2_load<KS>(f2, ap, bp, kn < KS ? kn : KS - 1); }
        if ((s & 3) == 3) { if (do_mma) g2_mma(f3, acc); if (s + 4 < NS) g2_load<KS>(f3, ap, bp, kn < KS ? kn : KS - 1); }
    }
    STAMP(kid, 2);
    constexpr int NOWN = (20 + NW - 1) / NW;
    f32x4 eo[NOWN];
    u32x2 ex[NOWN], ed[NOWN];
    float eg[NOWN];
#pragma unroll
    for (int o = 0; o < NOWN; ++o) {
        int tau = wid + NW * o;
        if (tau > 19) tau = 19;
        int i = tau / 5, j = tau % 5;
        int t = mb * 64 + i * 16 + (lane & 15);
        int d = nb * 80 + j * 16 + (lane >> 4) * 4;
        eg[o] = g[t];
        if (MODE == 1) {
            eo[o] = *(const f32x4*)(out + (size_t)t * DM + d);
            ex[o] = *(const u32x2*)(x + (size_t)t * DM + d);
            ed[o] = *(const u32x2*)(dskip + d);
        }
    }
    f32x4* r4 = (f32x4*)red;
#pragma unroll
    for (int tau = 0; tau < 20; ++tau) {
        const int owner = tau % NW;
        if (wid != owner) {
            int wslot = wid < owner ? wid : wid - 1;
            r4[(tau * (NW - 1) + wslot) * 64 + lane] = acc[tau / 5][tau % 5];
        }
    }
    __syncthreads();
#pragma unroll
    for (int tau = 0; tau < 20; ++tau) {
        if (wid == tau % NW) {
            const int o = tau / NW;
            f32x4 sacc = acc[tau / 5][tau % 5];
#pragma unroll
            for (int w = 0; w < NW - 1; ++w) sacc += r4[(tau * (NW - 1) + w) * 64 + lane];
            const int i = tau / 5, j = tau % 5;
            int t = mb * 64 + i * 16 + (lane & 15);
            int d = nb * 80 + j * 16 + (lane >> 4) * 4;
            f32x4 res;
            if (MODE == 0) {
                float omg = bfr(1.0f - eg[o]);
#pragma unroll
                for (int r = 0; r < 4; ++r) res[r] = bfr(omg * bfr(sacc[r]));
            } else {
                float s0 = bfr(bflo(ex[o][0]) * bflo(ed[o][0]));
                float s1 = bfr(bfhi(ex[o][0]) * bfhi(ed[o][0]));
                float s2 = bfr(bflo(ex[o][1]) * bflo(ed[o][1]));
                float s3 = bfr(bfhi(ex[o][1]) * bfhi(ed[o][1]));
                res[0] = eg[o] * (sacc[0] + s0) + eo[o][0];
                res[1] = eg[o] * (sacc[1] + s1) + eo[o][1];
                res[2] = eg[o] * (sacc[2] + s2) + eo[o][2];
                res[3] = eg[o] * (sacc[3] + s3) + eo[o][3];
            }
            *(f32x4*)(out + (size_t)t * DM + d) = res;
        }
    }
    STAMP(kid, 3);
    STAMP_DRAIN(kid, 4);
}

#define K2_FIX_BLOCKS (NCHUNK * 5)
#define K2_GEMM_BLOCKS 256
struct K2Params {
    const unsigned short* lre;
    const unsigned short* lim;
    const unsigned short* ldt;
    char* ws;
    float* out;
};

__global__ __launch_bounds__(256, 2) void k2_kernel(K2Params p) {
    __shared__ __attribute__((aligned(16))) float smem[16384];
    const int tid = threadIdx.x, lane = tid & 63, wid = tid >> 6;
    int bid = blockIdx.x;
    if (bid >= K2_FIX_BLOCKS) {
        bid -= K2_FIX_BLOCKS;
        int xcd = bid & 7, idx = bid >> 3;
        int mb = xcd * 4 + (idx >> 3), nb = idx & 7;
        gemm2_body<KS_MLP, 4, 0>((const u32x4*)(p.ws + WS_HIDA), (const u32x4*)(p.ws + WS_W2T), mb, nb, smem,
                                 (const float*)(p.ws + WS_G), nullptr, nullptr, p.out, p.ws, 1);
        return;
    }
    {
        const int c = bid / 5, cg = bid % 5;
        f32x2* corr = (f32x2*)smem;
        const f32x4* local = (const f32x4*)(p.ws + WS_LOCAL);
        const int tl = wid * 16 + (lane & 15);
        const int q = lane >> 4;
        const int t = c * 64 + tl;
        f32x4 lv[8][2];
#pragma unroll
        for (int k8 = 0; k8 < 8; ++k8) {
            int ch = cg * 128 + k8 * 16 + 4 * q;
            lv[k8][0] = local[((size_t)t * PN + ch) / 2];
            lv[k8][1] = local[((size_t)t * PN + ch) / 2 + 1];
        }
        {
            const int chl = tid & 127, part = tid >> 7;
            const int ch = cg * 128 + chl;
            const f32x2* agg = (const f32x2*)(p.ws + WS_AGG);
            float Lr, Li;
            unsigned short a0 = p.lre[ch], a1 = p.lim[ch], a2 = p.ldt[ch];
            f32x2 e[NCHUNK - 1];
#pragma unroll
            for (int cc = 0; cc < NCHUNK - 1; ++cc) {
                int ci2 = cc < c ? cc : 0;
                e[cc] = agg[(size_t)ci2 * PN + ch];
            }
            chan_lam(bf2f(a0), bf2f(a1), bf2f(a2), Lr, Li);
            float Pr = Lr, Pi = Li;
#pragma unroll
            for (int i = 0; i < 5; ++i) {
                float nr = Pr * Pr - Pi * Pi, ni = 2.f * Pr * Pi;
                Pr = nr; Pi = ni;
            }
            const float Qr = Pr, Qi = Pi;
            Pr = Qr * Qr - Qi * Qi; Pi = 2.f * Qr * Qi;
            float Hr = 0.f, Hi = 0.f;
#pragma unroll
            for (int cc = 0; cc < NCHUNK - 1; ++cc) {
                if (cc < c) {
                    float nr = Pr * Hr - Pi * Hi + e[cc][0];
                    float ni = Pr * Hi + Pi * Hr + e[cc][1];
                    Hr = nr; Hi = ni;
                }
            }
            if (part) { float nr = Qr * Hr - Qi * Hi, ni = Qr * Hi + Qi * Hr; Hr = nr; Hi = ni; }
#pragma unroll 8
            for (int n = 0; n < 32; ++n) {
                float nr = Lr * Hr - Li * Hi, ni = Lr * Hi + Li * Hr;
                Hr = nr; Hi = ni;
                corr[(part * 32 + n) * 128 + chl] = (f32x2){Hr, Hi};
            }
        }
        __syncthreads();
        u32x4* ha = (u32x4*)(p.ws + WS_HA);
#pragma unroll
        for (int k8 = 0; k8 < 8; ++k8) {
            int chl = k8 * 16 + 4 * q;
            f32x4 c0 = *(const f32x4*)&corr[tl * 128 + chl];
            f32x4 c1 = *(const f32x4*)&corr[tl * 128 + chl + 2];
            f32x4 v0 = lv[k8][0] + c0, v1 = lv[k8][1] + c1;
            u32x4 pk;
            pk[0] = pk2(v0[0], v0[1]);
            pk[1] = pk2(v0[2], v0[3]);
            pk[2] = pk2(v1[0], v1[1]);
            pk[3] = pk2(v1[2], v1[3]);
            int ks = cg * 8 + k8, mt = t >> 4;
            ha[((size_t)mt * KS_SSM + ks) * 64 + lane] = pk;
        }
    }
}

struct K3Params {
    const unsigned short* x;
    const unsigned short* dskip;
    char* ws;
    float* out;
};

__global__ __launch_bounds__(512, 2) void k3_kernel(K3Params p) {
    __shared__ __attribute__((aligned(16))) float smem[20 * 7 * 256];
    int bid = blockIdx.x;
    int xcd = bid & 7, idx = bid >> 3;
    int mb = xcd * 4 + (idx >> 3), nb = idx & 7;
    gemm2_body<KS_SSM, 8, 1>((const u32x4*)(p.ws + WS_HA), (const u32x4*)(p.ws + WS_CT), mb, nb, smem,
                             (const float*)(p.ws + WS_G), p.x, p.dskip, p.out, p.ws, 2);
}

extern "C" void kernel_launch(void* const* d_in, const int* in_sizes, int n_in, void* d_out, int out_size,
                              void* d_ws, size_t ws_size, hipStream_t stream) {
    const unsigned short* x = (const unsigned short*)d_in[0];
    const unsigned short* lre = (const unsigned short*)d_in[1];
    const unsigned short* lim = (const unsigned short*)d_in[2];
    const unsigned short* bre = (const unsigned short*)d_in[3];
    const unsigned short* bim = (const unsigned short*)d_in[4];
    const unsigned short* cre = (const unsigned short*)d_in[5];
    const unsigned short* cim = (const unsigned short*)d_in[6];
    const unsigned short* ldt = (const unsigned short*)d_in[7];
    const unsigned short* dskip = (const unsigned short*)d_in[8];
    const unsigned short* w1 = (const unsigned short*)d_in[10];
    const unsigned short* w2 = (const unsigned short*)d_in[11];
    const unsigned short* gw = (const unsigned short*)d_in[12];
    char* ws = (char*)d_ws;
    float* out = (float*)d_out;

    K1Params p1{x, lre, lim, bre, bim, cre, cim, ldt, w1, w2, gw, ws};
    k1_kernel<<<K1_GRID, 256, 0, stream>>>(p1);
    K2Params p2{lre, lim, ldt, ws, out};
    k2_kernel<<<K2_GEMM_BLOCKS + K2_FIX_BLOCKS, 256, 0, stream>>>(p2);
    K3Params p3{x, dskip, ws, out};
    k3_kernel<<<256, 512, 0, stream>>>(p3);
}
```

```cpp
#include <hip/hip_runtime.h>
#include <hip/hip_bf16.h>
#include <stdint.h>

typedef __attribute__((ext_vector_type(8))) __bf16 bf16x8;
typedef __attribute__((ext_vector_type(4))) __bf16 bf16x4;
typedef __attribute__((ext_vector_type(16))) float f32x16;
typedef __attribute__((ext_vector_type(4))) float f32x4;
typedef __attribute__((ext_vector_type(2))) float f32x2;
typedef __attribute__((ext_vector_type(4))) unsigned u32x4;
typedef __attribute__((ext_vector_type(2))) unsigned u32x2;
typedef __attribute__((address_space(3))) bf16x4* lds_bf16x4_ptr;

#define LSEQ 2048
#define DM 640
#define PN 640
#define FF 1728
#define CHUNK 64
#define NCHUNK (LSEQ / CHUNK)
#define KS_MLP (FF / 32)
#define KS_SSM (2 * PN / 32)
#define MT (LSEQ / 16)
#define NT (DM / 16)

#define WS_LOCAL 0
#define WS_LOCAL_SZ ((size_t)LSEQ * PN * 8)
#define WS_AGG (WS_LOCAL + WS_LOCAL_SZ)
#define WS_AGG_SZ ((size_t)NCHUNK * PN * 8)
#define WS_HIDA (WS_AGG + WS_AGG_SZ)
#define WS_HIDA_SZ ((size_t)MT * KS_MLP * 1024)
#define WS_HA (WS_HIDA + WS_HIDA_SZ)
#define WS_HA_SZ ((size_t)MT * KS_SSM * 1024)
#define WS_W2T (WS_HA + WS_HA_SZ)
#define WS_W2T_SZ ((size_t)NT * KS_MLP * 1024)
#define WS_CT (WS_W2T + WS_W2T_SZ)
#define WS_CT_SZ ((size_t)NT * KS_SSM * 1024)
#define WS_G (WS_CT + WS_CT_SZ)

#define STAMP(kid, idx) do {} while (0)
#define STAMP_DRAIN(kid, idx) do {} while (0)
#define WSTAMP(kid, idx) do {} while (0)

__device__ __forceinline__ float bf2f(unsigned short u) { return __uint_as_float(((unsigned)u) << 16); }
__device__ __forceinline__ float bflo(unsigned u) { return __uint_as_float(u << 16); }
__device__ __forceinline__ float bfhi(unsigned u) { return __uint_as_float(u & 0xffff0000u); }
__device__ __forceinline__ float bfr(float x) { return (float)(__bf16)x; }
__device__ __forceinline__ unsigned short f2bf(float x) {
    __bf16 b = (__bf16)x;
    return __builtin_bit_cast(unsigned short, b);
}
__device__ __forceinline__ unsigned pk2(float a, float b) { return f2bf(a) | ((unsigned)f2bf(b) << 16); }

__device__ __forceinline__ void sincos_acc(float x, float& s, float& c) {
    float kf = rintf(x * 0.63661977236758134f);
    float r = fmaf(kf, -1.5707962513e+00f, x);
    r = fmaf(kf, -7.5497894159e-08f, r);
    r = fmaf(kf, -5.3903029534e-15f, r);
    int k = (int)kf;
    float r2 = r * r;
    float sp = fmaf(r2, -1.9515295891e-04f, 8.3321608736e-03f);
    sp = fmaf(sp, r2, -1.6666654611e-01f);
    sp = fmaf(sp * r2, r, r);
    float cp = fmaf(r2, 2.443315711809948e-05f, -1.388731625493765e-03f);
    cp = fmaf(cp, r2, 4.166664568298827e-02f);
    cp = fmaf(cp * r2, r2, fmaf(-0.5f, r2, 1.0f));
    float ss = (k & 1) ? cp : sp;
    float cc = (k & 1) ? sp : cp;
    s = (k & 2) ? -ss : ss;
    c = ((k + 1) & 2) ? -cc : cc;
}

__device__ __forceinline__ void chan_lam(float lre, float lim, float ldt, float& Lr, float& Li) {
    float dt = bfr(expf(ldt));
    float a = dt * lre, b = dt * lim;
    float ea = expf(a);
    float sb, cb;
    sincos_acc(b, sb, cb);
    Lr = ea * cb;
    Li = ea * sb;
}
__device__ __forceinline__ void chan_consts(float lre, float lim, float ldt, float& Lr, float& Li,
                                            float& cr, float& ci) {
    float dt = bfr(expf(ldt));
    float a = dt * lre, b = dt * lim;
    float ea = expf(a);
    float sb, cb;
    sincos_acc(b, sb, cb);
    Lr = ea * cb;
    Li = ea * sb;
    float sh, chh;
    sincos_acc(0.5f * b, sh, chh);
    float nr = expm1f(a) * cb - 2.0f * sh * sh;
    float ni = Li;
    float den = lre * lre + lim * lim;
    float inv = 1.0f / den;
    cr = (nr * lre + ni * lim) * inv;
    ci = (ni * lre - nr * lim) * inv;
}

#define CMUL_R(ar, ai, br, bi) ((ar) * (br) - (ai) * (bi))
#define CMUL_I(ar, ai, br, bi) ((ar) * (bi) + (ai) * (br))

#define K1_GEMM_BLOCKS 192
#define K1_G_BLOCKS 64
#define K1_W2_BLOCKS 135
#define K1_C_BLOCKS 100
#define K1_GRID (K1_GEMM_BLOCKS + K1_G_BLOCKS + K1_W2_BLOCKS + K1_C_BLOCKS)

__device__ __forceinline__ unsigned swz128(unsigned row, unsigned ch) {
    return row * 128u + ((ch ^ ((row >> 1) & 7u)) << 4);
}
__device__ __forceinline__ unsigned off_b(unsigned row, unsigned ch) {
    return 256u * row + 16u * (ch ^ (((row & 3u) << 2) | ((row >> 2) & 3u)));
}

struct K1Params {
    const unsigned short* x;
    const unsigned short* lre;
    const unsigned short* lim;
    const unsigned short* bre;
    const unsigned short* bim;
    const unsigned short* cre;
    const unsigned short* cim;
    const unsigned short* ldt;
    const unsigned short* w1;
    const unsigned short* w2;
    const unsigned short* gw;
    char* ws;
};

template <bool SSM>
__device__ __forceinline__ void k1_tile(const K1Params& p, char* smem, int mtile, int ntile) {
    const int tid = threadIdx.x;
    const int lane = tid & 63;
    const int wid = tid >> 6;
    const int t0 = mtile * 256;
    const int wt = wid >> 1, wn = wid & 1;
    char* stamp_base = p.ws;
    STAMP(0, 0);

    const unsigned short* xsrc = p.x + (size_t)(t0 + (tid >> 3)) * DM + (tid & 7) * 8;
    const unsigned short* wsrc0;
    const unsigned short* wsrc1;
    if (SSM) {
        int r = tid >> 3;
        const unsigned short* base = (r >> 5) ? p.bim : p.bre;
        wsrc0 = base + (size_t)(ntile * 64 + (r & 31)) * DM + (tid & 7) * 8;
        wsrc1 = wsrc0 + 32 * DM;
    } else {
        int n = ntile * 128 + (tid & 15) * 8;
        if (n > FF - 8) n = FF - 8;
        wsrc0 = p.w1 + (size_t)(tid >> 4) * FF + n;
        wsrc1 = wsrc0 + (size_t)32 * FF;
    }
    unsigned xoff[4], woff[2];
#pragma unroll
    for (int it = 0; it < 4; ++it) xoff[it] = swz128((tid >> 3) + 64 * it, tid & 7);
#pragma unroll
    for (int it = 0; it < 2; ++it) woff[it] = SSM ? xoff[it] : off_b((tid >> 4) + 32 * it, tid & 15);

    f32x16 acc[2][2];
#pragma unroll
    for (int i = 0; i < 2; ++i)
#pragma unroll
        for (int j = 0; j < 2; ++j)
#pragma unroll
            for (int r = 0; r < 16; ++r) acc[i][j][r] = 0.f;

    const unsigned l31 = lane & 31, lh = lane >> 5;
    const int ch = ntile * 64 + wn * 32 + l31;
    unsigned short c_lre = 0, c_lim = 0, c_ldt = 0;
    if (SSM) { c_lre = p.lre[ch]; c_lim = p.lim[ch]; c_ldt = p.ldt[ch]; }

    u32x4 xrA[4], wrA[2], xrB[4], wrB[2], xrC[4], wrC[2];
#define K1_LOAD(XR, WR, KT)                                                                \
    do {                                                                                   \
        XR[0] = *(const u32x4*)(xsrc + (KT) * 64);                                         \
        XR[1] = *(const u32x4*)(xsrc + (KT) * 64 + 64 * DM);                               \
        XR[2] = *(const u32x4*)(xsrc + (KT) * 64 + 128 * DM);                              \
        XR[3] = *(const u32x4*)(xsrc + (KT) * 64 + 192 * DM);                              \
        if (SSM) {                                                                         \
            WR[0] = *(const u32x4*)(wsrc0 + (KT) * 64);                                    \
            WR[1] = *(const u32x4*)(wsrc1 + (KT) * 64);                                    \
        } else {                                                                           \
            WR[0] = *(const u32x4*)(wsrc0 + (size_t)(KT) * 64 * FF);                       \
            WR[1] = *(const u32x4*)(wsrc1 + (size_t)(KT) * 64 * FF);                       \
        }                                                                                  \
    } while (0)
    K1_LOAD(xrA, wrA, 0);
    K1_LOAD(xrB, wrB, 1);
    K1_LOAD(xrC, wrC, 2);
    __builtin_amdgcn_sched_barrier(0);

    float Lr = 0.f, Li = 0.f, cr = 0.f, ci = 0.f;
    if (SSM) chan_consts(bf2f(c_lre), bf2f(c_lim), bf2f(c_ldt), Lr, Li, cr, ci);
    __builtin_amdgcn_sched_barrier(0);

    const unsigned xa0 = wt * 64 + l31, xa1 = xa0 + 32;
    const unsigned ba0 = wn * 64 + l31, ba1 = ba0 + 32;
    const unsigned tq = (lane & 15) >> 2, tp = lane & 3, tblk = (lane >> 4) & 1;

#define K1_STEP(XR, WR, KT)                                                                          \
    do {                                                                                             \
        char* xs = smem + ((KT) & 1) * 49152;                                                        \
        char* wsm = xs + 32768;                                                                      \
        _Pragma("unroll") for (int it = 0; it < 4; ++it) *(u32x4*)(xs + xoff[it]) = XR[it];          \
        _Pragma("unroll") for (int it = 0; it < 2; ++it) *(u32x4*)(wsm + woff[it]) = WR[it];         \
        __syncthreads();                                                                             \
        if ((KT) + 3 < 10) K1_LOAD(XR, WR, (KT) + 3);                                                \
        __builtin_amdgcn_sched_barrier(0);                                                           \
        _Pragma("unroll") for (int s = 0; s < 4; ++s) {                                              \
            bf16x8 xf0 = *(const bf16x8*)(xs + swz128(xa0, 2 * s + lh));                             \
            bf16x8 xf1 = *(const bf16x8*)(xs + swz128(xa1, 2 * s + lh));                             \
            if (SSM) {                                                                               \
                bf16x8 bf0 = *(const bf16x8*)(wsm + swz128(ba0, 2 * s + lh));                        \
                bf16x8 bf1 = *(const bf16x8*)(wsm + swz128(ba1, 2 * s + lh));                        \
                acc[0][0] = __builtin_amdgcn_mfma_f32_32x32x16_bf16(xf0, bf0, acc[0][0], 0, 0, 0);   \
                acc[0][1] = __builtin_amdgcn_mfma_f32_32x32x16_bf16(xf0, bf1, acc[0][1], 0, 0, 0);   \
                acc[1][0] = __builtin_amdgcn_mfma_f32_32x32x16_bf16(xf1, bf0, acc[1][0], 0, 0, 0);   \
                acc[1][1] = __builtin_amdgcn_mfma_f32_32x32x16_bf16(xf1, bf1, acc[1][1], 0, 0, 0);   \
            } else {                                                                                 \
                bf16x8 wf[2];                                                                        \
                _Pragma("unroll") for (int i = 0; i < 2; ++i) {                                      \
                    unsigned c = 2 * wn + i;                                                         \
                    unsigned a0 = off_b(16 * s + 8 * lh + 0 + tq, 4 * c + 2 * tblk + (tp >> 1)) + 8 * (tp & 1); \
                    unsigned a1 = off_b(16 * s + 8 * lh + 4 + tq, 4 * c + 2 * tblk + (tp >> 1)) + 8 * (tp & 1); \
                    bf16x4 t0v = __builtin_amdgcn_ds_read_tr16_b64_v4bf16((lds_bf16x4_ptr)(wsm + a0)); \
                    bf16x4 t1v = __builtin_amdgcn_ds_read_tr16_b64_v4bf16((lds_bf16x4_ptr)(wsm + a1)); \
                    wf[i] = __builtin_shufflevector(t0v, t1v, 0, 1, 2, 3, 4, 5, 6, 7);               \
                }                                                                                    \
                acc[0][0] = __builtin_amdgcn_mfma_f32_32x32x16_bf16(wf[0], xf0, acc[0][0], 0, 0, 0); \
                acc[0][1] = __builtin_amdgcn_mfma_f32_32x32x16_bf16(wf[0], xf1, acc[0][1], 0, 0, 0); \
                acc[1][0] = __builtin_amdgcn_mfma_f32_32x32x16_bf16(wf[1], xf0, acc[1][0], 0, 0, 0); \
                acc[1][1] = __builtin_amdgcn_mfma_f32_32x32x16_bf16(wf[1], xf1, acc[1][1], 0, 0, 0); \
            }                                                                                        \
        }                                                                                            \
    } while (0)
    K1_STEP(xrA, wrA, 0);
    K1_STEP(xrB, wrB, 1);
    K1_STEP(xrC, wrC, 2);
    K1_STEP(xrA, wrA, 3);
    K1_STEP(xrB, wrB, 4);
    K1_STEP(xrC, wrC, 5);
    K1_STEP(xrA, wrA, 6);
    K1_STEP(xrB, wrB, 7);
    K1_STEP(xrC, wrC, 8);
    K1_STEP(xrA, wrA, 9);
#undef K1_STEP
#undef K1_LOAD
    STAMP(0, SSM ? 1 : 2);

    if (SSM) {
        float L2r = CMUL_R(Lr, Li, Lr, Li), L2i = CMUL_I(Lr, Li, Lr, Li);
        float L3r = CMUL_R(L2r, L2i, Lr, Li), L3i = CMUL_I(L2r, L2i, Lr, Li);
        float L4r = CMUL_R(L2r, L2i, L2r, L2i), L4i = CMUL_I(L2r, L2i, L2r, L2i);
        f32x16 vr[2], vi[2];
#pragma unroll
        for (int mi = 0; mi < 2; ++mi)
#pragma unroll
            for (int r = 0; r < 16; ++r) {
                float ar = acc[mi][0][r], ai = acc[mi][1][r];
                vr[mi][r] = cr * ar - ci * ai;
                vi[mi][r] = cr * ai + ci * ar;
            }
#pragma unroll
        for (int mi = 0; mi < 2; ++mi)
#pragma unroll
            for (int j = 0; j < 4; ++j)
#pragma unroll
                for (int i = 1; i < 4; ++i) {
                    int b = 4 * j + i;
                    float pr = vr[mi][b - 1], pi = vi[mi][b - 1];
                    vr[mi][b] += Lr * pr - Li * pi;
                    vi[mi][b] += Lr * pi + Li * pr;
                }
        float cR = 0.f, cI = 0.f;
#pragma unroll
        for (int mi = 0; mi < 2; ++mi)
#pragma unroll
            for (int j = 0; j < 4; ++j) {
                unsigned er = __float_as_uint(vr[mi][4 * j + 3]), ei = __float_as_uint(vi[mi][4 * j + 3]);
                auto sr = __builtin_amdgcn_permlane32_swap(er, er, false, false);
                auto si = __builtin_amdgcn_permlane32_swap(ei, ei, false, false);
                float E0r = __uint_as_float(sr[0]), E0i = __uint_as_float(si[0]);
                float E1r = __uint_as_float(sr[1]), E1i = __uint_as_float(si[1]);
                float c0r = cR, c0i = cI;
                float c1r = L4r * cR - L4i * cI + E0r;
                float c1i = L4r * cI + L4i * cR + E0i;
                cR = L4r * c1r - L4i * c1i + E1r;
                cI = L4r * c1i + L4i * c1r + E1i;
                float inr = lh ? c1r : c0r, ini = lh ? c1i : c0i;
                vr[mi][4 * j + 0] += Lr * inr - Li * ini;
                vi[mi][4 * j + 0] += Lr * ini + Li * inr;
                vr[mi][4 * j + 1] += L2r * inr - L2i * ini;
                vi[mi][4 * j + 1] += L2r * ini + L2i * inr;
                vr[mi][4 * j + 2] += L3r * inr - L3i * ini;
                vi[mi][4 * j + 2] += L3r * ini + L3i * inr;
                vr[mi][4 * j + 3] += L4r * inr - L4i * ini;
                vi[mi][4 * j + 3] += L4r * ini + L4i * inr;
            }
        f32x4* local = (f32x4*)(p.ws + WS_LOCAL);
#pragma unroll
        for (int mi = 0; mi < 2; ++mi)
#pragma unroll
            for (int j = 0; j < 4; ++j) {
                int tq4 = (t0 + wt * 64 + mi * 32 + 8 * j + 4 * lh) >> 2;
                size_t o = ((size_t)tq4 * PN + ch) * 2;
                local[o] = (f32x4){vr[mi][4 * j], vi[mi][4 * j], vr[mi][4 * j + 1], vi[mi][4 * j + 1]};
                local[o + 1] = (f32x4){vr[mi][4 * j + 2], vi[mi][4 * j + 2], vr[mi][4 * j + 3], vi[mi][4 * j + 3]};
            }
        if (lh == 0) {
            f32x2* agg = (f32x2*)(p.ws + WS_AGG);
            agg[(size_t)((t0 >> 6) + wt) * PN + ch] = (f32x2){cR, cI};
        }
        STAMP(0, 3);
        STAMP_DRAIN(0, 4);
    } else {
        u32x4* hid = (u32x4*)(p.ws + WS_HIDA);
#pragma unroll
        for (int fi = 0; fi < 2; ++fi) {
            int f0 = ntile * 128 + wn * 64 + fi * 32;
            if (f0 < FF) {
                int ks = f0 >> 5;
#pragma unroll
                for (int mi = 0; mi < 2; ++mi) {
                    int t = t0 + wt * 64 + mi * 32 + l31;
                    int mt = t >> 4;
#pragma unroll
                    for (int half = 0; half < 2; ++half) {
                        u32x4 pk;
#pragma unroll
                        for (int j = 0; j < 4; ++j) {
                            float v0 = acc[fi][mi][8 * half + 2 * j];
                            float v1 = acc[fi][mi][8 * half + 2 * j + 1];
                            v0 = v0 > 0.f ? v0 : 0.f;
                            v1 = v1 > 0.f ? v1 : 0.f;
                            pk[j] = pk2(v0, v1);
                        }
                        int q = 2 * half + lh;
                        hid[((size_t)mt * KS_MLP + ks) * 64 + (t & 15) + 16 * q] = pk;
                    }
                }
            }
        }
        STAMP_DRAIN(0, 5);
    }
}

__global__ __launch_bounds__(512, 2) void k1_kernel(K1Params p) {
    __shared__ __attribute__((aligned(16))) char smem[98304];
    const int tid = threadIdx.x;
    const int lane = tid & 63;
    const int wid = tid >> 6;
    int bid = blockIdx.x;

    if (bid < K1_GEMM_BLOCKS) {
        int xcd = bid & 7, idx = bid >> 3;
        int nt24 = xcd * 3 + idx / 8, mtile = idx % 8;
        if (nt24 < 10) k1_tile<true>(p, smem, mtile, nt24);
        else k1_tile<false>(p, smem, mtile, nt24 - 10);
        return;
    }
    bid -= K1_GEMM_BLOCKS;
    if (bid < K1_G_BLOCKS) {
        float* g = (float*)(p.ws + WS_G);
        const int t = (bid * 8 + wid) * 4 + (lane >> 4);
        const int l16 = lane & 15;
        u32x4 xv[5], gv[5];
#pragma unroll
        for (int i = 0; i < 5; ++i) {
            xv[i] = *(const u32x4*)(p.x + (size_t)t * DM + (l16 + 16 * i) * 8);
            gv[i] = *(const u32x4*)(p.gw + (l16 + 16 * i) * 8);
        }
        float sum = 0.f;
#pragma unroll
        for (int i = 0; i < 5; ++i)
#pragma unroll
            for (int e = 0; e < 4; ++e) {
                sum += bfr(bflo(xv[i][e]) * bflo(gv[i][e]));
                sum += bfr(bfhi(xv[i][e]) * bfhi(gv[i][e]));
            }
#pragma unroll
        for (int o = 8; o > 0; o >>= 1) sum += __shfl_xor(sum, o);
        if (l16 == 0) {
            float z = bfr(sum);
            g[t] = bfr(1.0f / (1.0f + expf(-z)));
        }
        return;
    }
    bid -= K1_G_BLOCKS;
    if (bid < K1_W2_BLOCKS) {
        u32x4* w2t = (u32x4*)(p.ws + WS_W2T);
        u32x4 pk[2];
#pragma unroll
        for (int i = 0; i < 2; ++i) {
            int u = (bid * 2 + i) * 512 + tid;
            int l = u & 63, fr = u >> 6;
            int ks = fr % KS_MLP, nt = fr / KS_MLP;
            int q = l >> 4, n = nt * 16 + (l & 15);
#pragma unroll
            for (int j = 0; j < 4; ++j) {
                int f = 32 * ks + 16 * (q >> 1) + 8 * (j >> 1) + 4 * (q & 1) + 2 * (j & 1);
                pk[i][j] = p.w2[(size_t)f * DM + n] | ((unsigned)p.w2[(size_t)(f + 1) * DM + n] << 16);
            }
        }
#pragma unroll
        for (int i = 0; i < 2; ++i) w2t[(bid * 2 + i) * 512 + tid] = pk[i];
        return;
    }
    bid -= K1_W2_BLOCKS;
    {
        u32x4* ct = (u32x4*)(p.ws + WS_CT);
        u32x2 re[2], im[2];
#pragma unroll
        for (int i = 0; i < 2; ++i) {
            int u = (bid * 2 + i) * 512 + tid;
            int l = u & 63, fr = u >> 6;
            int ks = fr % KS_SSM, nt = fr / KS_SSM;
            int q = l >> 4, d = nt * 16 + (l & 15);
            int pc = 16 * ks + 4 * q;
            re[i] = *(const u32x2*)(p.cre + (size_t)d * PN + pc);
            im[i] = *(const u32x2*)(p.cim + (size_t)d * PN + pc);
        }
#pragma unroll
        for (int i = 0; i < 2; ++i) {
            u32x4 pk;
            pk[0] = pk2(2.0f * bflo(re[i][0]), -2.0f * bflo(im[i][0]));
            pk[1] = pk2(2.0f * bfhi(re[i][0]), -2.0f * bfhi(im[i][0]));
            pk[2] = pk2(2.0f * bflo(re[i][1]), -2.0f * bflo(im[i][1]));
            pk[3] = pk2(2.0f * bfhi(re[i][1]), -2.0f * bfhi(im[i][1]));
            ct[(bid * 2 + i) * 512 + tid] = pk;
        }
    }
}

struct Frag9 { u32x4 a[4]; u32x4 b[5]; };

template <int KS>
__device__ __forceinline__ void g2_load(Frag9& f, const u32x4* __restrict__ ap, const u32x4* __restrict__ bp, int ks) {
#pragma unroll
    for (int i = 0; i < 4; ++i) f.a[i] = ap[((size_t)i * KS + ks) * 64];
#pragma unroll
    for (int j = 0; j < 5; ++j) f.b[j] = bp[((size_t)j * KS + ks) * 64];
    __builtin_amdgcn_sched_barrier(0);
}
__device__ __forceinline__ void g2_mma(const Frag9& f, f32x4 (&acc)[4][5]) {
#pragma unroll
    for (int i = 0; i < 4; ++i)
#pragma unroll
        for (int j = 0; j < 5; ++j)
            acc[i][j] = __builtin_amdgcn_mfma_f32_16x16x32_bf16(__builtin_bit_cast(bf16x8, f.b[j]),
                                                                __builtin_bit_cast(bf16x8, f.a[i]), acc[i][j], 0, 0, 0);
    __builtin_amdgcn_sched_barrier(0);
}

template <int KS, int NW, int MODE>
__device__ __forceinline__ void gemm2_body(const u32x4* __restrict__ A, const u32x4* __restrict__ B,
                                           int mb, int nb, float* red, const float* __restrict__ g,
                                           const unsigned short* __restrict__ x,
                                           const unsigned short* __restrict__ dskip, float* __restrict__ out,
                                           char* stamp_base, int kid) {
    STAMP(kid, 0);
    const int lane = threadIdx.x & 63;
    const int wid = __builtin_amdgcn_readfirstlane(threadIdx.x >> 6);
    constexpr int NS = (KS + NW - 1) / NW;
    f32x4 acc[4][5];
#pragma unroll
    for (int i = 0; i < 4; ++i)
#pragma unroll
        for (int j = 0; j < 5; ++j) acc[i][j] = (f32x4){0.f, 0.f, 0.f, 0.f};
    const u32x4* ap = A + ((size_t)(mb * 4) * KS) * 64 + lane;
    const u32x4* bp = B + ((size_t)(nb * 5) * KS) * 64 + lane;
    Frag9 f0, f1, f2, f3;
    g2_load<KS>(f0, ap, bp, wid);
    g2_load<KS>(f1, ap, bp, wid + NW);
    g2_load<KS>(f2, ap, bp, wid + 2 * NW);
    g2_load<KS>(f3, ap, bp, wid + 3 * NW);
    STAMP(kid, 1);
#pragma unroll
    for (int s = 0; s < NS; ++s) {
        const int kn = wid + NW * (s + 4);
        const bool last_ragged = (s == NS - 1) && (KS % NW != 0);
        const bool do_mma = !last_ragged || (wid + NW * s < KS);
        if ((s & 3) == 0) { if (do_mma) g2_mma(f0, acc); if (s + 4 < NS) g2_load<KS>(f0, ap, bp, kn < KS ? kn : KS - 1); }
        if ((s & 3) == 1) { if (do_mma) g2_mma(f1, acc); if (s + 4 < NS) g2_load<KS>(f1, ap, bp, kn < KS ? kn : KS - 1); }
        if ((s & 3) == 2) { if (do_mma) g2_mma(f2, acc); if (s + 4 < NS) g2_load<KS>(f2, ap, bp, kn < KS ? kn : KS - 1); }
        if ((s & 3) == 3) { if (do_mma) g2_mma(f3, acc); if (s + 4 < NS) g2_load<KS>(f3, ap, bp, kn < KS ? kn : KS - 1); }
    }
    STAMP(kid, 2);
    WSTAMP(kid, 0);
    constexpr int NOWN = (20 + NW - 1) / NW;
    f32x4 eo[NOWN];
    u32x2 ex[NOWN], ed[NOWN];
    float eg[NOWN];
#pragma unroll
    for (int o = 0; o < NOWN; ++o) {
        int tau = wid + NW * o;
        if (tau > 19) tau = 19;
        int i = tau / 5, j = tau % 5;
        int t = mb * 64 + i * 16 + (lane & 15);
        int d = nb * 80 + j * 16 + (lane >> 4) * 4;
        eg[o] = g[t];
        if (MODE == 1) {
            eo[o] = *(const f32x4*)(out + (size_t)t * DM + d);
            ex[o] = *(const u32x2*)(x + (size_t)t * DM + d);
            ed[o] = *(const u32x2*)(dskip + d);
        }
    }
    f32x4* r4 = (f32x4*)red;
#pragma unroll
    for (int tau = 0; tau < 20; ++tau) {
        const int owner = tau % NW;
        if (wid != owner) {
            int wslot = wid < owner ? wid : wid - 1;
            r4[(tau * (NW - 1) + wslot) * 64 + lane] = acc[tau / 5][tau % 5];
        }
    }
    WSTAMP(kid, 1);
    __syncthreads();
    WSTAMP(kid, 2);
#pragma unroll
    for (int tau = 0; tau < 20; ++tau) {
        if (wid == tau % NW) {
            const int o = tau / NW;
            f32x4 sacc = acc[tau / 5][tau % 5];
#pragma unroll
            for (int w = 0; w < NW - 1; ++w) sacc += r4[(tau * (NW - 1) + w) * 64 + lane];
            const int i = tau / 5, j = tau % 5;
            int t = mb * 64 + i * 16 + (lane & 15);
            int d = nb * 80 + j * 16 + (lane >> 4) * 4;
            f32x4 res;
            if (MODE == 0) {
                float omg = bfr(1.0f - eg[o]);
#pragma unroll
                for (int r = 0; r < 4; ++r) res[r] = bfr(omg * bfr(sacc[r]));
            } else {
                float s0 = bfr(bflo(ex[o][0]) * bflo(ed[o][0]));
                float s1 = bfr(bfhi(ex[o][0]) * bfhi(ed[o][0]));
                float s2 = bfr(bflo(ex[o][1]) * bflo(ed[o][1]));
                float s3 = bfr(bfhi(ex[o][1]) * bfhi(ed[o][1]));
                res[0] = eg[o] * (sacc[0] + s0) + eo[o][0];
                res[1] = eg[o] * (sacc[1] + s1) + eo[o][1];
                res[2] = eg[o] * (sacc[2] + s2) + eo[o][2];
                res[3] = eg[o] * (sacc[3] + s3) + eo[o][3];
            }
            *(f32x4*)(out + (size_t)t * DM + d) = res;
        }
    }
    WSTAMP(kid, 3);
    STAMP(kid, 3);
    STAMP_DRAIN(kid, 4);
}

#define K2_FIX_BLOCKS (NCHUNK * 5)
#define K2_GEMM_BLOCKS 256
struct K2Params {
    const unsigned short* lre;
    const unsigned short* lim;
    const unsigned short* ldt;
    char* ws;
    float* out;
};

__global__ __launch_bounds__(256, 2) void k2_kernel(K2Params p) {
    __shared__ __attribute__((aligned(16))) float smem[16384];
    const int tid = threadIdx.x, lane = tid & 63, wid = tid >> 6;
    int bid = blockIdx.x;
    if (bid >= K2_FIX_BLOCKS) {
        bid -= K2_FIX_BLOCKS;
        int xcd = bid & 7, idx = bid >> 3;
        int mb = xcd * 4 + (idx >> 3), nb = idx & 7;
        gemm2_body<KS_MLP, 4, 0>((const u32x4*)(p.ws + WS_HIDA), (const u32x4*)(p.ws + WS_W2T), mb, nb, smem,
                                 (const float*)(p.ws + WS_G), nullptr, nullptr, p.out, p.ws, 1);
        return;
    }
    {
        const int c = bid / 5, cg = bid % 5;
        f32x2* corr = (f32x2*)smem;
        const f32x4* local = (const f32x4*)(p.ws + WS_LOCAL);
        f32x4 lv[2][8];
#pragma unroll
        for (int u = 0; u < 2; ++u) {
            int uid = tid + 256 * u;
            int cq = uid & 31, tq4 = uid >> 5;
            size_t o = ((size_t)(c * 16 + tq4) * PN + cg * 128 + cq * 4) * 2;
#pragma unroll
            for (int e = 0; e < 8; ++e) lv[u][e] = local[o + e];
        }
        {
            const int chl = tid & 127, part = tid >> 7;
            const int ch = cg * 128 + chl;
            const f32x2* agg = (const f32x2*)(p.ws + WS_AGG);
            float Lr, Li;
            unsigned short a0 = p.lre[ch], a1 = p.lim[ch], a2 = p.ldt[ch];
            f32x2 e[NCHUNK - 1];
#pragma unroll
            for (int cc = 0; cc < NCHUNK - 1; ++cc) {
                int ci2 = cc < c ? cc : 0;
                e[cc] = agg[(size_t)ci2 * PN + ch];
            }
            chan_lam(bf2f(a0), bf2f(a1), bf2f(a2), Lr, Li);
            float Pr = Lr, Pi = Li;
#pragma unroll
            for (int i = 0; i < 5; ++i) {
                float nr = Pr * Pr - Pi * Pi, ni = 2.f * Pr * Pi;
                Pr = nr; Pi = ni;
            }
            const float Qr = Pr, Qi = Pi;
            Pr = Qr * Qr - Qi * Qi; Pi = 2.f * Qr * Qi;
            float Hr = 0.f, Hi = 0.f;
#pragma unroll
            for (int cc = 0; cc < NCHUNK - 1; ++cc) {
                if (cc < c) {
                    float nr = Pr * Hr - Pi * Hi + e[cc][0];
                    float ni = Pr * Hi + Pi * Hr + e[cc][1];
                    Hr = nr; Hi = ni;
                }
            }
            if (part) { float nr = Qr * Hr - Qi * Hi, ni = Qr * Hi + Qi * Hr; Hr = nr; Hi = ni; }
#pragma unroll 8
            for (int n = 0; n < 32; ++n) {
                float nr = Lr * Hr - Li * Hi, ni = Lr * Hi + Li * Hr;
                Hr = nr; Hi = ni;
                corr[(part * 32 + n) * 128 + chl] = (f32x2){Hr, Hi};
            }
        }
        __syncthreads();
        u32x4* ha = (u32x4*)(p.ws + WS_HA);
#pragma unroll
        for (int u = 0; u < 2; ++u) {
            int uid = tid + 256 * u;
            int cq = uid & 31, tq4 = uid >> 5;
            int chl = cq * 4;
#pragma unroll
            for (int tt = 0; tt < 4; ++tt) {
                int tl = tq4 * 4 + tt;
                f32x4 c0 = *(const f32x4*)&corr[tl * 128 + chl];
                f32x4 c1 = *(const f32x4*)&corr[tl * 128 + chl + 2];
                u32x4 pk;
                pk[0] = pk2(lv[u][0 + (tt >> 1)][2 * (tt & 1)] + c0[0], lv[u][0 + (tt >> 1)][2 * (tt & 1) + 1] + c0[1]);
                pk[1] = pk2(lv[u][2 + (tt >> 1)][2 * (tt & 1)] + c0[2], lv[u][2 + (tt >> 1)][2 * (tt & 1) + 1] + c0[3]);
                pk[2] = pk2(lv[u][4 + (tt >> 1)][2 * (tt & 1)] + c1[0], lv[u][4 + (tt >> 1)][2 * (tt & 1) + 1] + c1[1]);
                pk[3] = pk2(lv[u][6 + (tt >> 1)][2 * (tt & 1)] + c1[2], lv[u][6 + (tt >> 1)][2 * (tt & 1) + 1] + c1[3]);
                int t = c * 64 + tl;
                int ks = cg * 8 + (cq >> 2), q = cq & 3, mt = t >> 4;
                ha[((size_t)mt * KS_SSM + ks) * 64 + (t & 15) + 16 * q] = pk;
            }
        }
    }
}

struct K3Params {
    const unsigned short* x;
    const unsigned short* dskip;
    char* ws;
    float* out;
};

__global__ __launch_bounds__(256, 2) void k3_kernel(K3Params p) {
    __shared__ __attribute__((aligned(16))) float smem[20 * 3 * 256];
    int bid = blockIdx.x;
    int xcd = bid & 7, idx = bid >> 3;
    int mb = xcd * 4 + (idx >> 3), nb = idx & 7;
    gemm2_body<KS_SSM, 4, 1>((const u32x4*)(p.ws + WS_HA), (const u32x4*)(p.ws + WS_CT), mb, nb, smem,
                             (const float*)(p.ws + WS_G), p.x, p.dskip, p.out, p.ws, 2);
}

extern "C" void kernel_launch(void* const* d_in, const int* in_sizes, int n_in, void* d_out, int out_size,
                              void* d_ws, size_t ws_size, hipStream_t stream) {
    const unsigned short* x = (const unsigned short*)d_in[0];
    const unsigned short* lre = (const unsigned short*)d_in[1];
    const unsigned short* lim = (const unsigned short*)d_in[2];
    const unsigned short* bre = (const unsigned short*)d_in[3];
    const unsigned short* bim = (const unsigned short*)d_in[4];
    const unsigned short* cre = (const unsigned short*)d_in[5];
    const unsigned short* cim = (const unsigned short*)d_in[6];
    const unsigned short* ldt = (const unsigned short*)d_in[7];
    const unsigned short* dskip = (const unsigned short*)d_in[8];
    const unsigned short* w1 = (const unsigned short*)d_in[10];
    const unsigned short* w2 = (const unsigned short*)d_in[11];
    const unsigned short* gw = (const unsigned short*)d_in[12];
    char* ws = (char*)d_ws;
    float* out = (float*)d_out;

    K1Params p1{x, lre, lim, bre, bim, cre, cim, ldt, w1, w2, gw, ws};
    k1_kernel<<<K1_GRID, 512, 0, stream>>>(p1);
    K2Params p2{lre, lim, ldt, ws, out};
    k2_kernel<<<K2_GEMM_BLOCKS + K2_FIX_BLOCKS, 256, 0, stream>>>(p2);
    K3Params p3{x, dskip, ws, out};
    k3_kernel<<<256, 256, 0, stream>>>(p3);
}
```

```cpp
#include <hip/hip_runtime.h>
#include <hip/hip_bf16.h>
#include <stdint.h>

typedef __attribute__((ext_vector_type(8))) __bf16 bf16x8;
typedef __attribute__((ext_vector_type(4))) __bf16 bf16x4;
typedef __attribute__((ext_vector_type(16))) float f32x16;
typedef __attribute__((ext_vector_type(4))) float f32x4;
typedef __attribute__((ext_vector_type(2))) float f32x2;
typedef __attribute__((ext_vector_type(4))) unsigned u32x4;
typedef __attribute__((ext_vector_type(2))) unsigned u32x2;
typedef __attribute__((address_space(3))) bf16x4* lds_bf16x4_ptr;

#define LSEQ 2048
#define DM 640
#define PN 640
#define FF 1728
#define CHUNK 64
#define NCHUNK (LSEQ / CHUNK)
#define KS_MLP (FF / 32)
#define KS_SSM (2 * PN / 32)
#define MT (LSEQ / 16)
#define NT (DM / 16)

#define WS_LOCAL 0
#define WS_LOCAL_SZ ((size_t)LSEQ * PN * 8)
#define WS_AGG (WS_LOCAL + WS_LOCAL_SZ)
#define WS_AGG_SZ ((size_t)NCHUNK * PN * 8)
#define WS_HIDA (WS_AGG + WS_AGG_SZ)
#define WS_HIDA_SZ ((size_t)MT * KS_MLP * 1024)
#define WS_HA (WS_HIDA + WS_HIDA_SZ)
#define WS_HA_SZ ((size_t)MT * KS_SSM * 1024)
#define WS_W2T (WS_HA + WS_HA_SZ)
#define WS_W2T_SZ ((size_t)NT * KS_MLP * 1024)
#define WS_CT (WS_W2T + WS_W2T_SZ)
#define WS_CT_SZ ((size_t)NT * KS_SSM * 1024)
#define WS_G (WS_CT + WS_CT_SZ)

#define STAMP(kid, idx) do {} while (0)
#define STAMP_DRAIN(kid, idx) do {} while (0)
#define WSTAMP(kid, idx) do {} while (0)

__device__ __forceinline__ float bf2f(unsigned short u) { return __uint_as_float(((unsigned)u) << 16); }
__device__ __forceinline__ float bflo(unsigned u) { return __uint_as_float(u << 16); }
__device__ __forceinline__ float bfhi(unsigned u) { return __uint_as_float(u & 0xffff0000u); }
__device__ __forceinline__ float bfr(float x) { return (float)(__bf16)x; }
__device__ __forceinline__ unsigned short f2bf(float x) {
    __bf16 b = (__bf16)x;
    return __builtin_bit_cast(unsigned short, b);
}
__device__ __forceinline__ unsigned pk2(float a, float b) { return f2bf(a) | ((unsigned)f2bf(b) << 16); }

__device__ __forceinline__ void sincos_acc(float x, float& s, float& c) {
    float kf = rintf(x * 0.63661977236758134f);
    float r = fmaf(kf, -1.5707962513e+00f, x);
    r = fmaf(kf, -7.5497894159e-08f, r);
    r = fmaf(kf, -5.3903029534e-15f, r);
    int k = (int)kf;
    float r2 = r * r;
    float sp = fmaf(r2, -1.9515295891e-04f, 8.3321608736e-03f);
    sp = fmaf(sp, r2, -1.6666654611e-01f);
    sp = fmaf(sp * r2, r, r);
    float cp = fmaf(r2, 2.443315711809948e-05f, -1.388731625493765e-03f);
    cp = fmaf(cp, r2, 4.166664568298827e-02f);
    cp = fmaf(cp * r2, r2, fmaf(-0.5f, r2, 1.0f));
    float ss = (k & 1) ? cp : sp;
    float cc = (k & 1) ? sp : cp;
    s = (k & 2) ? -ss : ss;
    c = ((k + 1) & 2) ? -cc : cc;
}

__device__ __forceinline__ void chan_lam(float lre, float lim, float ldt, float& Lr, float& Li) {
    float dt = bfr(expf(ldt));
    float a = dt * lre, b = dt * lim;
    float ea = expf(a);
    float sb, cb;
    sincos_acc(b, sb, cb);
    Lr = ea * cb;
    Li = ea * sb;
}
__device__ __forceinline__ void chan_consts(float lre, float lim, float ldt, float& Lr, float& Li,
                                            float& cr, float& ci) {
    float dt = bfr(expf(ldt));
    float a = dt * lre, b = dt * lim;
    float ea = expf(a);
    float sb, cb;
    sincos_acc(b, sb, cb);
    Lr = ea * cb;
    Li = ea * sb;
    float sh, chh;
    sincos_acc(0.5f * b, sh, chh);
    float nr = expm1f(a) * cb - 2.0f * sh * sh;
    float ni = Li;
    float den = lre * lre + lim * lim;
    float inv = 1.0f / den;
    cr = (nr * lre + ni * lim) * inv;
    ci = (ni * lre - nr * lim) * inv;
}

#define CMUL_R(ar, ai, br, bi) ((ar) * (br) - (ai) * (bi))
#define CMUL_I(ar, ai, br, bi) ((ar) * (bi) + (ai) * (br))

#define K1_GEMM_BLOCKS 192
#define K1_G_BLOCKS 32
#define K1_W2_BLOCKS 34
#define K1_C_BLOCKS 25
#define K1_GRID (K1_GEMM_BLOCKS + K1_G_BLOCKS + K1_W2_BLOCKS + K1_C_BLOCKS)

__device__ __forceinline__ unsigned swz128(unsigned row, unsigned ch) {
    return row * 128u + ((ch ^ ((row >> 1) & 7u)) << 4);
}
__device__ __forceinline__ unsigned off_b(unsigned row, unsigned ch) {
    return 256u * row + 16u * (ch ^ (((row & 3u) << 2) | ((row >> 2) & 3u)));
}

struct K1Params {
    const unsigned short* x;
    const unsigned short* lre;
    const unsigned short* lim;
    const unsigned short* bre;
    const unsigned short* bim;
    const unsigned short* cre;
    const unsigned short* cim;
    const unsigned short* ldt;
    const unsigned short* w1;
    const unsigned short* w2;
    const unsigned short* gw;
    char* ws;
};

template <bool SSM>
__device__ __forceinline__ void k1_tile(const K1Params& p, char* smem, int mtile, int ntile) {
    const int tid = threadIdx.x;
    const int lane = tid & 63;
    const int wid = tid >> 6;
    const int t0 = mtile * 256;
    const int wt = wid >> 1, wn = wid & 1;
    char* stamp_base = p.ws;
    STAMP(0, 0);

    const unsigned short* xsrc = p.x + (size_t)(t0 + (tid >> 3)) * DM + (tid & 7) * 8;
    const unsigned short* wsrc0;
    const unsigned short* wsrc1;
    if (SSM) {
        int r = tid >> 3;
        const unsigned short* base = (r >> 5) ? p.bim : p.bre;
        wsrc0 = base + (size_t)(ntile * 64 + (r & 31)) * DM + (tid & 7) * 8;
        wsrc1 = wsrc0 + 32 * DM;
    } else {
        int n = ntile * 128 + (tid & 15) * 8;
        if (n > FF - 8) n = FF - 8;
        wsrc0 = p.w1 + (size_t)(tid >> 4) * FF + n;
        wsrc1 = wsrc0 + (size_t)32 * FF;
    }
    unsigned xoff[4], woff[2];
#pragma unroll
    for (int it = 0; it < 4; ++it) xoff[it] = swz128((tid >> 3) + 64 * it, tid & 7);
#pragma unroll
    for (int it = 0; it < 2; ++it) woff[it] = SSM ? xoff[it] : off_b((tid >> 4) + 32 * it, tid & 15);

    f32x16 acc[2][2];
#pragma unroll
    for (int i = 0; i < 2; ++i)
#pragma unroll
        for (int j = 0; j < 2; ++j)
#pragma unroll
            for (int r = 0; r < 16; ++r) acc[i][j][r] = 0.f;

    const unsigned l31 = lane & 31, lh = lane >> 5;
    const int ch = ntile * 64 + wn * 32 + l31;
    unsigned short c_lre = 0, c_lim = 0, c_ldt = 0;
    if (SSM) { c_lre = p.lre[ch]; c_lim = p.lim[ch]; c_ldt = p.ldt[ch]; }

    u32x4 xrA[4], wrA[2], xrB[4], wrB[2], xrC[4], wrC[2];
#define K1_LOAD(XR, WR, KT)                                                                \
    do {                                                                                   \
        XR[0] = *(const u32x4*)(xsrc + (KT) * 64);                                         \
        XR[1] = *(const u32x4*)(xsrc + (KT) * 64 + 64 * DM);                               \
        XR[2] = *(const u32x4*)(xsrc + (KT) * 64 + 128 * DM);                              \
        XR[3] = *(const u32x4*)(xsrc + (KT) * 64 + 192 * DM);                              \
        if (SSM) {                                                                         \
            WR[0] = *(const u32x4*)(wsrc0 + (KT) * 64);                                    \
            WR[1] = *(const u32x4*)(wsrc1 + (KT) * 64);                                    \
        } else {                                                                           \
            WR[0] = *(const u32x4*)(wsrc0 + (size_t)(KT) * 64 * FF);                       \
            WR[1] = *(const u32x4*)(wsrc1 + (size_t)(KT) * 64 * FF);                       \
        }                                                                                  \
    } while (0)
    K1_LOAD(xrA, wrA, 0);
    K1_LOAD(xrB, wrB, 1);
    K1_LOAD(xrC, wrC, 2);
    __builtin_amdgcn_sched_barrier(0);

    float Lr = 0.f, Li = 0.f, cr = 0.f, ci = 0.f;
    if (SSM) chan_consts(bf2f(c_lre), bf2f(c_lim), bf2f(c_ldt), Lr, Li, cr, ci);
    __builtin_amdgcn_sched_barrier(0);

    const unsigned xa0 = wt * 64 + l31, xa1 = xa0 + 32;
    const unsigned ba0 = wn * 64 + l31, ba1 = ba0 + 32;
    const unsigned tq = (lane & 15) >> 2, tp = lane & 3, tblk = (lane >> 4) & 1;

#define K1_FRAGS(F, XS, WSM, S)                                                                      \
    do {                                                                                             \
        F[0] = *(const bf16x8*)((XS) + swz128(xa0, 2 * (S) + lh));                                   \
        F[1] = *(const bf16x8*)((XS) + swz128(xa1, 2 * (S) + lh));                                   \
        if (SSM) {                                                                                   \
            F[2] = *(const bf16x8*)((WSM) + swz128(ba0, 2 * (S) + lh));                              \
            F[3] = *(const bf16x8*)((WSM) + swz128(ba1, 2 * (S) + lh));                              \
        } else {                                                                                     \
            _Pragma("unroll") for (int i = 0; i < 2; ++i) {                                          \
                unsigned c = 2 * wn + i;                                                             \
                unsigned a0 = off_b(16 * (S) + 8 * lh + 0 + tq, 4 * c + 2 * tblk + (tp >> 1)) + 8 * (tp & 1); \
                unsigned a1 = off_b(16 * (S) + 8 * lh + 4 + tq, 4 * c + 2 * tblk + (tp >> 1)) + 8 * (tp & 1); \
                bf16x4 t0v = __builtin_amdgcn_ds_read_tr16_b64_v4bf16((lds_bf16x4_ptr)((WSM) + a0)); \
                bf16x4 t1v = __builtin_amdgcn_ds_read_tr16_b64_v4bf16((lds_bf16x4_ptr)((WSM) + a1)); \
                F[2 + i] = __builtin_shufflevector(t0v, t1v, 0, 1, 2, 3, 4, 5, 6, 7);                \
            }                                                                                        \
        }                                                                                            \
    } while (0)
#define K1_MMA(F)                                                                                    \
    do {                                                                                             \
        if (SSM) {                                                                                   \
            acc[0][0] = __builtin_amdgcn_mfma_f32_32x32x16_bf16(F[0], F[2], acc[0][0], 0, 0, 0);     \
            acc[0][1] = __builtin_amdgcn_mfma_f32_32x32x16_bf16(F[0], F[3], acc[0][1], 0, 0, 0);     \
            acc[1][0] = __builtin_amdgcn_mfma_f32_32x32x16_bf16(F[1], F[2], acc[1][0], 0, 0, 0);     \
            acc[1][1] = __builtin_amdgcn_mfma_f32_32x32x16_bf16(F[1], F[3], acc[1][1], 0, 0, 0);     \
        } else {                                                                                     \
            acc[0][0] = __builtin_amdgcn_mfma_f32_32x32x16_bf16(F[2], F[0], acc[0][0], 0, 0, 0);     \
            acc[0][1] = __builtin_amdgcn_mfma_f32_32x32x16_bf16(F[2], F[1], acc[0][1], 0, 0, 0);     \
            acc[1][0] = __builtin_amdgcn_mfma_f32_32x32x16_bf16(F[3], F[0], acc[1][0], 0, 0, 0);     \
            acc[1][1] = __builtin_amdgcn_mfma_f32_32x32x16_bf16(F[3], F[1], acc[1][1], 0, 0, 0);     \
        }                                                                                            \
    } while (0)
#define K1_WRITE(XR, WR, KT)                                                                         \
    do {                                                                                             \
        char* xs_ = smem + ((KT) & 1) * 49152;                                                       \
        _Pragma("unroll") for (int it = 0; it < 4; ++it) *(u32x4*)(xs_ + xoff[it]) = XR[it];         \
        _Pragma("unroll") for (int it = 0; it < 2; ++it) *(u32x4*)(xs_ + 32768 + woff[it]) = WR[it]; \
    } while (0)
#define K1_STEP(XN, WN, KT)                                                                          \
    do {                                                                                             \
        char* xs = smem + ((KT) & 1) * 49152;                                                        \
        char* wsm = xs + 32768;                                                                      \
        bf16x8 fA[4], fB[4];                                                                         \
        K1_FRAGS(fA, xs, wsm, 0);                                                                    \
        K1_FRAGS(fB, xs, wsm, 1);                                                                    \
        __builtin_amdgcn_sched_barrier(0);                                                           \
        K1_MMA(fA);                                                                                  \
        K1_FRAGS(fA, xs, wsm, 2);                                                                    \
        __builtin_amdgcn_sched_barrier(0);                                                           \
        K1_MMA(fB);                                                                                  \
        K1_FRAGS(fB, xs, wsm, 3);                                                                    \
        if ((KT) + 1 < 10) K1_WRITE(XN, WN, (KT) + 1);                                               \
        if ((KT) + 4 < 10) K1_LOAD(XN, WN, (KT) + 4);                                                \
        __builtin_amdgcn_sched_barrier(0);                                                           \
        K1_MMA(fA);                                                                                  \
        __builtin_amdgcn_sched_barrier(0);                                                           \
        K1_MMA(fB);                                                                                  \
        STAMP(0, 8 + 2 * (KT));                                                                      \
        if ((KT) + 1 < 10) __syncthreads();                                                          \
        STAMP(0, 9 + 2 * (KT));                                                                      \
    } while (0)
    K1_WRITE(xrA, wrA, 0);
    K1_LOAD(xrA, wrA, 3);
    __syncthreads();
    K1_STEP(xrB, wrB, 0);
    K1_STEP(xrC, wrC, 1);
    K1_STEP(xrA, wrA, 2);
    K1_STEP(xrB, wrB, 3);
    K1_STEP(xrC, wrC, 4);
    K1_STEP(xrA, wrA, 5);
    K1_STEP(xrB, wrB, 6);
    K1_STEP(xrC, wrC, 7);
    K1_STEP(xrA, wrA, 8);
    K1_STEP(xrB, wrB, 9);
#undef K1_FRAGS
#undef K1_MMA
#undef K1_WRITE
#undef K1_STEP
#undef K1_LOAD
    STAMP(0, SSM ? 1 : 2);

    if (SSM) {
        float L2r = CMUL_R(Lr, Li, Lr, Li), L2i = CMUL_I(Lr, Li, Lr, Li);
        float L3r = CMUL_R(L2r, L2i, Lr, Li), L3i = CMUL_I(L2r, L2i, Lr, Li);
        float L4r = CMUL_R(L2r, L2i, L2r, L2i), L4i = CMUL_I(L2r, L2i, L2r, L2i);
        f32x16 vr[2], vi[2];
#pragma unroll
        for (int mi = 0; mi < 2; ++mi)
#pragma unroll
            for (int r = 0; r < 16; ++r) {
                float ar = acc[mi][0][r], ai = acc[mi][1][r];
                vr[mi][r] = cr * ar - ci * ai;
                vi[mi][r] = cr * ai + ci * ar;
            }
#pragma unroll
        for (int mi = 0; mi < 2; ++mi)
#pragma unroll
            for (int j = 0; j < 4; ++j)
#pragma unroll
                for (int i = 1; i < 4; ++i) {
                    int b = 4 * j + i;
                    float pr = vr[mi][b - 1], pi = vi[mi][b - 1];
                    vr[mi][b] += Lr * pr - Li * pi;
                    vi[mi][b] += Lr * pi + Li * pr;
                }
        float cR = 0.f, cI = 0.f;
#pragma unroll
        for (int mi = 0; mi < 2; ++mi)
#pragma unroll
            for (int j = 0; j < 4; ++j) {
                unsigned er = __float_as_uint(vr[mi][4 * j + 3]), ei = __float_as_uint(vi[mi][4 * j + 3]);
                auto sr = __builtin_amdgcn_permlane32_swap(er, er, false, false);
                auto si = __builtin_amdgcn_permlane32_swap(ei, ei, false, false);
                float E0r = __uint_as_float(sr[0]), E0i = __uint_as_float(si[0]);
                float E1r = __uint_as_float(sr[1]), E1i = __uint_as_float(si[1]);
                float c0r = cR, c0i = cI;
                float c1r = L4r * cR - L4i * cI + E0r;
                float c1i = L4r * cI + L4i * cR + E0i;
                cR = L4r * c1r - L4i * c1i + E1r;
                cI = L4r * c1i + L4i * c1r + E1i;
                float inr = lh ? c1r : c0r, ini = lh ? c1i : c0i;
                vr[mi][4 * j + 0] += Lr * inr - Li * ini;
                vi[mi][4 * j + 0] += Lr * ini + Li * inr;
                vr[mi][4 * j + 1] += L2r * inr - L2i * ini;
                vi[mi][4 * j + 1] += L2r * ini + L2i * inr;
                vr[mi][4 * j + 2] += L3r * inr - L3i * ini;
                vi[mi][4 * j + 2] += L3r * ini + L3i * inr;
                vr[mi][4 * j + 3] += L4r * inr - L4i * ini;
                vi[mi][4 * j + 3] += L4r * ini + L4i * inr;
            }
        f32x4* local = (f32x4*)(p.ws + WS_LOCAL);
#pragma unroll
        for (int mi = 0; mi < 2; ++mi)
#pragma unroll
            for (int j = 0; j < 4; ++j) {
                int tq4 = (t0 + wt * 64 + mi * 32 + 8 * j + 4 * lh) >> 2;
                size_t o = ((size_t)tq4 * PN + ch) * 2;
                local[o] = (f32x4){vr[mi][4 * j], vi[mi][4 * j], vr[mi][4 * j + 1], vi[mi][4 * j + 1]};
                local[o + 1] = (f32x4){vr[mi][4 * j + 2], vi[mi][4 * j + 2], vr[mi][4 * j + 3], vi[mi][4 * j + 3]};
            }
        if (lh == 0) {
            f32x2* agg = (f32x2*)(p.ws + WS_AGG);
            agg[(size_t)((t0 >> 6) + wt) * PN + ch] = (f32x2){cR, cI};
        }
        STAMP(0, 3);
        STAMP_DRAIN(0, 4);
    } else {
        u32x4* hid = (u32x4*)(p.ws + WS_HIDA);
#pragma unroll
        for (int fi = 0; fi < 2; ++fi) {
            int f0 = ntile * 128 + wn * 64 + fi * 32;
            if (f0 < FF) {
                int ks = f0 >> 5;
#pragma unroll
                for (int mi = 0; mi < 2; ++mi) {
                    int t = t0 + wt * 64 + mi * 32 + l31;
                    int mt = t >> 4;
#pragma unroll
                    for (int half = 0; half < 2; ++half) {
                        u32x4 pk;
#pragma unroll
                        for (int j = 0; j < 4; ++j) {
                            float v0 = acc[fi][mi][8 * half + 2 * j];
                            float v1 = acc[fi][mi][8 * half + 2 * j + 1];
                            v0 = v0 > 0.f ? v0 : 0.f;
                            v1 = v1 > 0.f ? v1 : 0.f;
                            pk[j] = pk2(v0, v1);
                        }
                        int q = 2 * half + lh;
                        hid[((size_t)mt * KS_MLP + ks) * 64 + (t & 15) + 16 * q] = pk;
                    }
                }
            }
        }
        STAMP_DRAIN(0, 5);
    }
}

__global__ __launch_bounds__(512, 2) void k1_kernel(K1Params p) {
    __shared__ __attribute__((aligned(16))) char smem[98304];
    const int tid = threadIdx.x;
    const int lane = tid & 63;
    const int wid = tid >> 6;
    int bid = blockIdx.x;

    if (bid < K1_GEMM_BLOCKS) {
        int xcd = bid & 7, idx = bid >> 3;
        int nt24 = xcd * 3 + idx / 8, mtile = idx % 8;
        if (nt24 < 10) k1_tile<true>(p, smem, mtile, nt24);
        else k1_tile<false>(p, smem, mtile, nt24 - 10);
        return;
    }
    bid -= K1_GEMM_BLOCKS;
    if (bid < K1_G_BLOCKS) {
        float* g = (float*)(p.ws + WS_G);
        const int l16 = lane & 15;
        u32x4 xv[2][5], gv[5];
#pragma unroll
        for (int i = 0; i < 5; ++i) gv[i] = *(const u32x4*)(p.gw + (l16 + 16 * i) * 8);
#pragma unroll
        for (int u = 0; u < 2; ++u) {
            const int t = ((bid * 8 + wid) * 2 + u) * 4 + (lane >> 4);
#pragma unroll
            for (int i = 0; i < 5; ++i) xv[u][i] = *(const u32x4*)(p.x + (size_t)t * DM + (l16 + 16 * i) * 8);
        }
#pragma unroll
        for (int u = 0; u < 2; ++u) {
            const int t = ((bid * 8 + wid) * 2 + u) * 4 + (lane >> 4);
            float sum = 0.f;
#pragma unroll
            for (int i = 0; i < 5; ++i)
#pragma unroll
                for (int e = 0; e < 4; ++e) {
                    sum += bfr(bflo(xv[u][i][e]) * bflo(gv[i][e]));
                    sum += bfr(bfhi(xv[u][i][e]) * bfhi(gv[i][e]));
                }
#pragma unroll
            for (int o = 8; o > 0; o >>= 1) sum += __shfl_xor(sum, o);
            if (l16 == 0) {
                float z = bfr(sum);
                g[t] = bfr(1.0f / (1.0f + expf(-z)));
            }
        }
        return;
    }
    bid -= K1_G_BLOCKS;
    if (bid < K1_W2_BLOCKS) {
        u32x4* w2t = (u32x4*)(p.ws + WS_W2T);
        char* img = smem + wid * 8192;
        u32x4 v[2][4];
#pragma unroll
        for (int u = 0; u < 2; ++u) {
            int task = (bid * 8 + wid) * 2 + u;
            if (task > KS_MLP * 10 - 1) task = KS_MLP * 10 - 1;
            int ks = task / 10, ng = task % 10;
#pragma unroll
            for (int i = 0; i < 4; ++i) {
                int idx = lane + 64 * i, row = idx >> 3, c8 = idx & 7;
                v[u][i] = *(const u32x4*)(p.w2 + (size_t)(32 * ks + row) * DM + ng * 64 + c8 * 8);
            }
        }
#pragma unroll
        for (int u = 0; u < 2; ++u)
#pragma unroll
            for (int i = 0; i < 4; ++i) {
                int idx = lane + 64 * i, row = idx >> 3, c8 = idx & 7;
                *(u32x4*)(img + u * 4096 + row * 128 + c8 * 16) = v[u][i];
            }
        __builtin_amdgcn_fence(__ATOMIC_RELEASE, "wavefront");
        __builtin_amdgcn_wave_barrier();
        __builtin_amdgcn_fence(__ATOMIC_ACQUIRE, "wavefront");
        const int q = lane >> 4, qq = (lane & 15) >> 2, pp = lane & 3;
#pragma unroll
        for (int u = 0; u < 2; ++u) {
            int task = (bid * 8 + wid) * 2 + u;
            if (task < KS_MLP * 10) {
                int ks = task / 10, ng = task % 10;
#pragma unroll
                for (int c = 0; c < 4; ++c) {
                    int r0 = 16 * (q >> 1) + 4 * (q & 1);
                    unsigned a0 = u * 4096 + (r0 + qq) * 128 + c * 32 + pp * 8;
                    unsigned a1 = a0 + 8 * 128;
                    bf16x4 lo = __builtin_amdgcn_ds_read_tr16_b64_v4bf16((lds_bf16x4_ptr)(img + a0));
                    bf16x4 hi = __builtin_amdgcn_ds_read_tr16_b64_v4bf16((lds_bf16x4_ptr)(img + a1));
                    bf16x8 f = __builtin_shufflevector(lo, hi, 0, 1, 2, 3, 4, 5, 6, 7);
                    w2t[((size_t)(ng * 4 + c) * KS_MLP + ks) * 64 + lane] = __builtin_bit_cast(u32x4, f);
                }
            }
        }
        return;
    }
    bid -= K1_W2_BLOCKS;
    {
        u32x4* ct = (u32x4*)(p.ws + WS_CT);
        u32x4 re[4], im[4];
#pragma unroll
        for (int u = 0; u < 4; ++u) {
            int task = (bid * 8 + wid) * 4 + u;
            int nt = task / 20, pg = task % 20;
            int d = nt * 16 + (lane & 15), pc = pg * 32 + (lane >> 4) * 8;
            re[u] = *(const u32x4*)(p.cre + (size_t)d * PN + pc);
            im[u] = *(const u32x4*)(p.cim + (size_t)d * PN + pc);
        }
#pragma unroll
        for (int u = 0; u < 4; ++u) {
            int task = (bid * 8 + wid) * 4 + u;
            int nt = task / 20, pg = task % 20;
            int c = lane >> 4;
            int ks = pg * 2 + (c >> 1), q0 = 2 * (c & 1);
            u32x4 pa, pb;
            pa[0] = pk2(2.0f * bflo(re[u][0]), -2.0f * bflo(im[u][0]));
            pa[1] = pk2(2.0f * bfhi(re[u][0]), -2.0f * bfhi(im[u][0]));
            pa[2] = pk2(2.0f * bflo(re[u][1]), -2.0f * bflo(im[u][1]));
            pa[3] = pk2(2.0f * bfhi(re[u][1]), -2.0f * bfhi(im[u][1]));
            pb[0] = pk2(2.0f * bflo(re[u][2]), -2.0f * bflo(im[u][2]));
            pb[1] = pk2(2.0f * bfhi(re[u][2]), -2.0f * bfhi(im[u][2]));
            pb[2] = pk2(2.0f * bflo(re[u][3]), -2.0f * bflo(im[u][3]));
            pb[3] = pk2(2.0f * bfhi(re[u][3]), -2.0f * bfhi(im[u][3]));
            size_t o = ((size_t)nt * KS_SSM + ks) * 64 + (lane & 15);
            ct[o + 16 * q0] = pa;
            ct[o + 16 * (q0 + 1)] = pb;
        }
    }
}

struct Frag9 { u32x4 a[4]; u32x4 b[5]; };

template <int KS>
__device__ __forceinline__ void g2_load(Frag9& f, const u32x4* __restrict__ ap, const u32x4* __restrict__ bp, int ks) {
#pragma unroll
    for (int i = 0; i < 4; ++i) f.a[i] = ap[((size_t)i * KS + ks) * 64];
#pragma unroll
    for (int j = 0; j < 5; ++j) f.b[j] = bp[((size_t)j * KS + ks) * 64];
    __builtin_amdgcn_sched_barrier(0);
}
__device__ __forceinline__ void g2_mma(const Frag9& f, f32x4 (&acc)[4][5]) {
#pragma unroll
    for (int i = 0; i < 4; ++i)
#pragma unroll
        for (int j = 0; j < 5; ++j)
            acc[i][j] = __builtin_amdgcn_mfma_f32_16x16x32_bf16(__builtin_bit_cast(bf16x8, f.b[j]),
                                                                __builtin_bit_cast(bf16x8, f.a[i]), acc[i][j], 0, 0, 0);
    __builtin_amdgcn_sched_barrier(0);
}

template <int KS, int NW, int MODE>
__device__ __forceinline__ void gemm2_body(const u32x4* __restrict__ A, const u32x4* __restrict__ B,
                                           int mb, int nb, float* red, const float* __restrict__ g,
                                           const unsigned short* __restrict__ x,
                                           const unsigned short* __restrict__ dskip, float* __restrict__ out,
                                           char* stamp_base, int kid) {
    STAMP(kid, 0);
    const int lane = threadIdx.x & 63;
    const int wid = __builtin_amdgcn_readfirstlane(threadIdx.x >> 6);
    constexpr int NS = (KS + NW - 1) / NW;
    f32x4 acc[4][5];
#pragma unroll
    for (int i = 0; i < 4; ++i)
#pragma unroll
        for (int j = 0; j < 5; ++j) acc[i][j] = (f32x4){0.f, 0.f, 0.f, 0.f};
    const u32x4* ap = A + ((size_t)(mb * 4) * KS) * 64 + lane;
    const u32x4* bp = B + ((size_t)(nb * 5) * KS) * 64 + lane;
    Frag9 f0, f1, f2, f3;
    g2_load<KS>(f0, ap, bp, wid);
    g2_load<KS>(f1, ap, bp, wid + NW);
    g2_load<KS>(f2, ap, bp, wid + 2 * NW);
    g2_load<KS>(f3, ap, bp, wid + 3 * NW);
    STAMP(kid, 1);
#pragma unroll
    for (int s = 0; s < NS; ++s) {
        const int kn = wid + NW * (s + 4);
        const bool last_ragged = (s == NS - 1) && (KS % NW != 0);
        const bool do_mma = !last_ragged || (wid + NW * s < KS);
        if ((s & 3) == 0) { if (do_mma) g2_mma(f0, acc); if (s + 4 < NS) g2_load<KS>(f0, ap, bp, kn < KS ? kn : KS - 1); }
        if ((s & 3) == 1) { if (do_mma) g2_mma(f1, acc); if (s + 4 < NS) g2_load<KS>(f1, ap, bp, kn < KS ? kn : KS - 1); }
        if ((s & 3) == 2) { if (do_mma) g2_mma(f2, acc); if (s + 4 < NS) g2_load<KS>(f2, ap, bp, kn < KS ? kn : KS - 1); }
        if ((s & 3) == 3) { if (do_mma) g2_mma(f3, acc); if (s + 4 < NS) g2_load<KS>(f3, ap, bp, kn < KS ? kn : KS - 1); }
    }
    STAMP(kid, 2);
    WSTAMP(kid, 0);
    constexpr int NOWN = (20 + NW - 1) / NW;
    f32x4 eo[NOWN];
    u32x2 ex[NOWN], ed[NOWN];
    float eg[NOWN];
#pragma unroll
    for (int o = 0; o < NOWN; ++o) {
        int tau = wid + NW * o;
        if (tau > 19) tau = 19;
        int i = tau / 5, j = tau % 5;
        int t = mb * 64 + i * 16 + (lane & 15);
        int d = nb * 80 + j * 16 + (lane >> 4) * 4;
        eg[o] = g[t];
        if (MODE == 1) {
            eo[o] = *(const f32x4*)(out + (size_t)t * DM + d);
            ex[o] = *(const u32x2*)(x + (size_t)t * DM + d);
            ed[o] = *(const u32x2*)(dskip + d);
        }
    }
    f32x4* r4 = (f32x4*)red;
#pragma unroll
    for (int tau = 0; tau < 20; ++tau) {
        const int owner = tau % NW;
        if (wid != owner) {
            int wslot = wid < owner ? wid : wid - 1;
            r4[(tau * (NW - 1) + wslot) * 64 + lane] = acc[tau / 5][tau % 5];
        }
    }
    WSTAMP(kid, 1);
    __syncthreads();
    WSTAMP(kid, 2);
#pragma unroll
    for (int tau = 0; tau < 20; ++tau) {
        if (wid == tau % NW) {
            const int o = tau / NW;
            f32x4 sacc = acc[tau / 5][tau % 5];
#pragma unroll
            for (int w = 0; w < NW - 1; ++w) sacc += r4[(tau * (NW - 1) + w) * 64 + lane];
            const int i = tau / 5, j = tau % 5;
            int t = mb * 64 + i * 16 + (lane & 15);
            int d = nb * 80 + j * 16 + (lane >> 4) * 4;
            f32x4 res;
            if (MODE == 0) {
                float omg = bfr(1.0f - eg[o]);
#pragma unroll
                for (int r = 0; r < 4; ++r) res[r] = bfr(omg * bfr(sacc[r]));
            } else {
                float s0 = bfr(bflo(ex[o][0]) * bflo(ed[o][0]));
                float s1 = bfr(bfhi(ex[o][0]) * bfhi(ed[o][0]));
                float s2 = bfr(bflo(ex[o][1]) * bflo(ed[o][1]));
                float s3 = bfr(bfhi(ex[o][1]) * bfhi(ed[o][1]));
                res[0] = eg[o] * (sacc[0] + s0) + eo[o][0];
                res[1] = eg[o] * (sacc[1] + s1) + eo[o][1];
                res[2] = eg[o] * (sacc[2] + s2) + eo[o][2];
                res[3] = eg[o] * (sacc[3] + s3) + eo[o][3];
            }
            *(f32x4*)(out + (size_t)t * DM + d) = res;
        }
    }
    WSTAMP(kid, 3);
    STAMP(kid, 3);
    STAMP_DRAIN(kid, 4);
}

#define K2_FIX_BLOCKS (NCHUNK * 5)
#define K2_GEMM_BLOCKS 256
struct K2Params {
    const unsigned short* lre;
    const unsigned short* lim;
    const unsigned short* ldt;
    char* ws;
    float* out;
};

__global__ __launch_bounds__(256, 2) void k2_kernel(K2Params p) {
    __shared__ __attribute__((aligned(16))) float smem[16384];
    const int tid = threadIdx.x, lane = tid & 63, wid = tid >> 6;
    int bid = blockIdx.x;
    if (bid >= K2_FIX_BLOCKS) {
        bid -= K2_FIX_BLOCKS;
        int xcd = bid & 7, idx = bid >> 3;
        int mb = xcd * 4 + (idx >> 3), nb = idx & 7;
        gemm2_body<KS_MLP, 4, 0>((const u32x4*)(p.ws + WS_HIDA), (const u32x4*)(p.ws + WS_W2T), mb, nb, smem,
                                 (const float*)(p.ws + WS_G), nullptr, nullptr, p.out, p.ws, 1);
        return;
    }
    {
        const int c = bid / 5, cg = bid % 5;
        f32x2* corr = (f32x2*)smem;
        const f32x4* local = (const f32x4*)(p.ws + WS_LOCAL);
        f32x4 lv[2][8];
#pragma unroll
        for (int u = 0; u < 2; ++u) {
            int uid = tid + 256 * u;
            int cq = uid & 31, tq4 = uid >> 5;
            size_t o = ((size_t)(c * 16 + tq4) * PN + cg * 128 + cq * 4) * 2;
#pragma unroll
            for (int e = 0; e < 8; ++e) lv[u][e] = local[o + e];
        }
        {
            const int chl = tid & 127, part = tid >> 7;
            const int ch = cg * 128 + chl;
            const f32x2* agg = (const f32x2*)(p.ws + WS_AGG);
            float Lr, Li;
            unsigned short a0 = p.lre[ch], a1 = p.lim[ch], a2 = p.ldt[ch];
            f32x2 e[NCHUNK - 1];
#pragma unroll
            for (int cc = 0; cc < NCHUNK - 1; ++cc) {
                int ci2 = cc < c ? cc : 0;
                e[cc] = agg[(size_t)ci2 * PN + ch];
            }
            chan_lam(bf2f(a0), bf2f(a1), bf2f(a2), Lr, Li);
            float Pr = Lr, Pi = Li;
#pragma unroll
            for (int i = 0; i < 5; ++i) {
                float nr = Pr * Pr - Pi * Pi, ni = 2.f * Pr * Pi;
                Pr = nr; Pi = ni;
            }
            const float Qr = Pr, Qi = Pi;
            Pr = Qr * Qr - Qi * Qi; Pi = 2.f * Qr * Qi;
            float Hr = 0.f, Hi = 0.f;
#pragma unroll
            for (int cc = 0; cc < NCHUNK - 1; ++cc) {
                if (cc < c) {
                    float nr = Pr * Hr - Pi * Hi + e[cc][0];
                    float ni = Pr * Hi + Pi * Hr + e[cc][1];
                    Hr = nr; Hi = ni;
                }
            }
            if (part) { float nr = Qr * Hr - Qi * Hi, ni = Qr * Hi + Qi * Hr; Hr = nr; Hi = ni; }
#pragma unroll 8
            for (int n = 0; n < 32; ++n) {
                float nr = Lr * Hr - Li * Hi, ni = Lr * Hi + Li * Hr;
                Hr = nr; Hi = ni;
                corr[(part * 32 + n) * 128 + chl] = (f32x2){Hr, Hi};
            }
        }
        __syncthreads();
        u32x4* ha = (u32x4*)(p.ws + WS_HA);
#pragma unroll
        for (int u = 0; u < 2; ++u) {
            int uid = tid + 256 * u;
            int cq = uid & 31, tq4 = uid >> 5;
            int chl = cq * 4;
#pragma unroll
            for (int tt = 0; tt < 4; ++tt) {
                int tl = tq4 * 4 + tt;
                f32x4 c0 = *(const f32x4*)&corr[tl * 128 + chl];
                f32x4 c1 = *(const f32x4*)&corr[tl * 128 + chl + 2];
                u32x4 pk;
                pk[0] = pk2(lv[u][0 + (tt >> 1)][2 * (tt & 1)] + c0[0], lv[u][0 + (tt >> 1)][2 * (tt & 1) + 1] + c0[1]);
                pk[1] = pk2(lv[u][2 + (tt >> 1)][2 * (tt & 1)] + c0[2], lv[u][2 + (tt >> 1)][2 * (tt & 1) + 1] + c0[3]);
                pk[2] = pk2(lv[u][4 + (tt >> 1)][2 * (tt & 1)] + c1[0], lv[u][4 + (tt >> 1)][2 * (tt & 1) + 1] + c1[1]);
                pk[3] = pk2(lv[u][6 + (tt >> 1)][2 * (tt & 1)] + c1[2], lv[u][6 + (tt >> 1)][2 * (tt & 1) + 1] + c1[3]);
                int t = c * 64 + tl;
                int ks = cg * 8 + (cq >> 2), q = cq & 3, mt = t >> 4;
                ha[((size_t)mt * KS_SSM + ks) * 64 + (t & 15) + 16 * q] = pk;
            }
        }
    }
}

struct K3Params {
    const unsigned short* x;
    const unsigned short* dskip;
    char* ws;
    float* out;
};

__global__ __launch_bounds__(256, 2) void k3_kernel(K3Params p) {
    __shared__ __attribute__((aligned(16))) float smem[20 * 3 * 256];
    int bid = blockIdx.x;
    int xcd = bid & 7, idx = bid >> 3;
    int mb = xcd * 4 + (idx >> 3), nb = idx & 7;
    gemm2_body<KS_SSM, 4, 1>((const u32x4*)(p.ws + WS_HA), (const u32x4*)(p.ws + WS_CT), mb, nb, smem,
                             (const float*)(p.ws + WS_G), p.x, p.dskip, p.out, p.ws, 2);
}

extern "C" void kernel_launch(void* const* d_in, const int* in_sizes, int n_in, void* d_out, int out_size,
                              void* d_ws, size_t ws_size, hipStream_t stream) {
    const unsigned short* x = (const unsigned short*)d_in[0];
    const unsigned short* lre = (const unsigned short*)d_in[1];
    const unsigned short* lim = (const unsigned short*)d_in[2];
    const unsigned short* bre = (const unsigned short*)d_in[3];
    const unsigned short* bim = (const unsigned short*)d_in[4];
    const unsigned short* cre = (const unsigned short*)d_in[5];
    const unsigned short* cim = (const unsigned short*)d_in[6];
    const unsigned short* ldt = (const unsigned short*)d_in[7];
    const unsigned short* dskip = (const unsigned short*)d_in[8];
    const unsigned short* w1 = (const unsigned short*)d_in[10];
    const unsigned short* w2 = (const unsigned short*)d_in[11];
    const unsigned short* gw = (const unsigned short*)d_in[12];
    char* ws = (char*)d_ws;
    float* out = (float*)d_out;

    K1Params p1{x, lre, lim, bre, bim, cre, cim, ldt, w1, w2, gw, ws};
    k1_kernel<<<K1_GRID, 512, 0, stream>>>(p1);
    K2Params p2{lre, lim, ldt, ws, out};
    k2_kernel<<<K2_GEMM_BLOCKS + K2_FIX_BLOCKS, 256, 0, stream>>>(p2);
    K3Params p3{x, dskip, ws, out};
    k3_kernel<<<256, 256, 0, stream>>>(p3);
}
```

```cpp
#include <hip/hip_runtime.h>
#include <hip/hip_bf16.h>
#include <stdint.h>

typedef __attribute__((ext_vector_type(8))) __bf16 bf16x8;
typedef __attribute__((ext_vector_type(4))) __bf16 bf16x4;
typedef __attribute__((ext_vector_type(16))) float f32x16;
typedef __attribute__((ext_vector_type(4))) float f32x4;
typedef __attribute__((ext_vector_type(2))) float f32x2;
typedef __attribute__((ext_vector_type(4))) unsigned u32x4;
typedef __attribute__((ext_vector_type(2))) unsigned u32x2;
typedef __attribute__((address_space(3))) bf16x4* lds_bf16x4_ptr;

#define LSEQ 2048
#define DM 640
#define PN 640
#define FF 1728
#define CHUNK 64
#define NCHUNK (LSEQ / CHUNK)
#define KS_MLP (FF / 32)
#define KS_SSM (2 * PN / 32)
#define MT (LSEQ / 16)
#define NT (DM / 16)

#define WS_LOCAL 0
#define WS_LOCAL_SZ ((size_t)LSEQ * PN * 8)
#define WS_AGG (WS_LOCAL + WS_LOCAL_SZ)
#define WS_AGG_SZ ((size_t)NCHUNK * PN * 8)
#define WS_HIDA (WS_AGG + WS_AGG_SZ)
#define WS_HIDA_SZ ((size_t)MT * KS_MLP * 1024)
#define WS_HA (WS_HIDA + WS_HIDA_SZ)
#define WS_HA_SZ ((size_t)MT * KS_SSM * 1024)
#define WS_W2T (WS_HA + WS_HA_SZ)
#define WS_W2T_SZ ((size_t)NT * KS_MLP * 1024)
#define WS_CT (WS_W2T + WS_W2T_SZ)
#define WS_CT_SZ ((size_t)NT * KS_SSM * 1024)
#define WS_G (WS_CT + WS_CT_SZ)

#define STAMP(kid, idx) do {} while (0)
#define STAMP_DRAIN(kid, idx) do {} while (0)
#define WSTAMP(kid, idx) do {} while (0)

__device__ __forceinline__ float bf2f(unsigned short u) { return __uint_as_float(((unsigned)u) << 16); }
__device__ __forceinline__ float bflo(unsigned u) { return __uint_as_float(u << 16); }
__device__ __forceinline__ float bfhi(unsigned u) { return __uint_as_float(u & 0xffff0000u); }
__device__ __forceinline__ float bfr(float x) { return (float)(__bf16)x; }
__device__ __forceinline__ unsigned short f2bf(float x) {
    __bf16 b = (__bf16)x;
    return __builtin_bit_cast(unsigned short, b);
}
#define NT_STORES 0
template <typename T>
__device__ __forceinline__ void st_out(T* ptr, T v) {
    *ptr = v;
}
__device__ __forceinline__ unsigned pk2(float a, float b) { return f2bf(a) | ((unsigned)f2bf(b) << 16); }

__device__ __forceinline__ void sincos_acc(float x, float& s, float& c) {
    float kf = rintf(x * 0.63661977236758134f);
    float r = fmaf(kf, -1.5707962513e+00f, x);
    r = fmaf(kf, -7.5497894159e-08f, r);
    r = fmaf(kf, -5.3903029534e-15f, r);
    int k = (int)kf;
    float r2 = r * r;
    float sp = fmaf(r2, -1.9515295891e-04f, 8.3321608736e-03f);
    sp = fmaf(sp, r2, -1.6666654611e-01f);
    sp = fmaf(sp * r2, r, r);
    float cp = fmaf(r2, 2.443315711809948e-05f, -1.388731625493765e-03f);
    cp = fmaf(cp, r2, 4.166664568298827e-02f);
    cp = fmaf(cp * r2, r2, fmaf(-0.5f, r2, 1.0f));
    float ss = (k & 1) ? cp : sp;
    float cc = (k & 1) ? sp : cp;
    s = (k & 2) ? -ss : ss;
    c = ((k + 1) & 2) ? -cc : cc;
}

__device__ __forceinline__ void chan_lam(float lre, float lim, float ldt, float& Lr, float& Li) {
    float dt = bfr(expf(ldt));
    float a = dt * lre, b = dt * lim;
    float ea = expf(a);
    float sb, cb;
    sincos_acc(b, sb, cb);
    Lr = ea * cb;
    Li = ea * sb;
}
__device__ __forceinline__ void chan_consts(float lre, float lim, float ldt, float& Lr, float& Li,
                                            float& cr, float& ci) {
    float dt = bfr(expf(ldt));
    float a = dt * lre, b = dt * lim;
    float ea = expf(a);
    float sb, cb;
    sincos_acc(b, sb, cb);
    Lr = ea * cb;
    Li = ea * sb;
    float sh, chh;
    sincos_acc(0.5f * b, sh, chh);
    float nr = expm1f(a) * cb - 2.0f * sh * sh;
    float ni = Li;
    float den = lre * lre + lim * lim;
    float inv = 1.0f / den;
    cr = (nr * lre + ni * lim) * inv;
    ci = (ni * lre - nr * lim) * inv;
}

#define CMUL_R(ar, ai, br, bi) ((ar) * (br) - (ai) * (bi))
#define CMUL_I(ar, ai, br, bi) ((ar) * (bi) + (ai) * (br))

#define K1_GEMM_BLOCKS 192
#define K1_G_BLOCKS 32
#define K1_W2_BLOCKS 34
#define K1_C_BLOCKS 25
#define K1_GRID (K1_GEMM_BLOCKS + K1_G_BLOCKS + K1_W2_BLOCKS + K1_C_BLOCKS)

__device__ __forceinline__ unsigned swz128(unsigned row, unsigned ch) {
    return row * 128u + ((ch ^ ((row >> 1) & 7u)) << 4);
}
__device__ __forceinline__ unsigned off_b(unsigned row, unsigned ch) {
    return 256u * row + 16u * (ch ^ (((row & 3u) << 2) | ((row >> 2) & 3u)));
}

struct K1Params {
    const unsigned short* x;
    const unsigned short* lre;
    const unsigned short* lim;
    const unsigned short* bre;
    const unsigned short* bim;
    const unsigned short* cre;
    const unsigned short* cim;
    const unsigned short* ldt;
    const unsigned short* w1;
    const unsigned short* w2;
    const unsigned short* gw;
    char* ws;
};

__device__ __forceinline__ bf16x4 tr_read_asm(unsigned lds_addr) {
    bf16x4 r;
    asm volatile("ds_read_b64_tr_b16 %0, %1" : "=v"(r) : "v"(lds_addr) : "memory");
    return r;
}
__device__ __forceinline__ unsigned lds_addr_of(const char* ptr) {
    return (unsigned)(size_t)(__attribute__((address_space(3))) const char*)ptr;
}
#define K1_STAGE_BYTES 49152
#define K1_CONST_OFF (3 * K1_STAGE_BYTES)
#define RAW_BARRIER() do { asm volatile("s_waitcnt lgkmcnt(0)" ::: "memory"); __builtin_amdgcn_s_barrier(); } while (0)

template <bool SSM>
__device__ __forceinline__ void k1_tile(const K1Params& p, char* smem, int mtile, int ntile) {
    const int tid = threadIdx.x;
    const int lane = tid & 63;
    const int wid = __builtin_amdgcn_readfirstlane(tid >> 6);
    const int t0 = mtile * 256;
    const int wt = wid >> 1, wn = wid & 1;
    char* stamp_base = p.ws;
    STAMP(0, 0);

    const unsigned short* xs_src[4];
#pragma unroll
    for (int i = 0; i < 4; ++i) {
        unsigned row = (4 * wid + i) * 8 + (lane >> 3);
        unsigned chk = (lane & 7) ^ ((row >> 1) & 7);
        xs_src[i] = p.x + (size_t)(t0 + row) * DM + chk * 8;
    }
    const unsigned short* ws_src[2];
#pragma unroll
    for (int i = 0; i < 2; ++i) {
        if (SSM) {
            unsigned row = (2 * wid + i) * 8 + (lane >> 3);
            unsigned chk = (lane & 7) ^ ((row >> 1) & 7);
            unsigned grp = row >> 5;
            const unsigned short* base = (grp & 1) ? p.bim : p.bre;
            ws_src[i] = base + (size_t)(ntile * 64 + (row & 31) + 32 * (grp >> 1)) * DM + chk * 8;
        } else {
            unsigned row = (2 * wid + i) * 4 + (lane >> 4);
            unsigned chk = (lane & 15) ^ (((row & 3u) << 2) | ((row >> 2) & 3u));
            int n = ntile * 128 + chk * 8;
            if (n > FF - 8) n = FF - 8;
            ws_src[i] = p.w1 + (size_t)row * FF + n;
        }
    }
#define K1_GLDS(KT)                                                                                  \
    do {                                                                                             \
        char* st_ = smem + ((KT) % 3) * K1_STAGE_BYTES;                                              \
        _Pragma("unroll") for (int i = 0; i < 4; ++i)                                                \
            __builtin_amdgcn_global_load_lds((const unsigned*)(xs_src[i] + (KT) * 64),               \
                                             (unsigned*)(st_ + (4 * wid + i) * 1024), 16, 0, 0);     \
        _Pragma("unroll") for (int i = 0; i < 2; ++i)                                                \
            __builtin_amdgcn_global_load_lds((const unsigned*)(ws_src[i] + (SSM ? (size_t)(KT) * 64 : (size_t)(KT) * 64 * FF)), \
                                             (unsigned*)(st_ + 32768 + (2 * wid + i) * 1024), 16, 0, 0); \
    } while (0)

    if (SSM) {
        const int cc = ntile * 64 + 2 * (lane & 31);
        __builtin_amdgcn_global_load_lds((const unsigned*)(p.lre + cc), (unsigned*)(smem + K1_CONST_OFF), 4, 0, 0);
        __builtin_amdgcn_global_load_lds((const unsigned*)(p.lim + cc), (unsigned*)(smem + K1_CONST_OFF + 256), 4, 0, 0);
        __builtin_amdgcn_global_load_lds((const unsigned*)(p.ldt + cc), (unsigned*)(smem + K1_CONST_OFF + 512), 4, 0, 0);
    }
    K1_GLDS(0);
    K1_GLDS(1);

    f32x16 acc[2][2];
#pragma unroll
    for (int i = 0; i < 2; ++i)
#pragma unroll
        for (int j = 0; j < 2; ++j)
#pragma unroll
            for (int r = 0; r < 16; ++r) acc[i][j][r] = 0.f;

    const unsigned l31 = lane & 31, lh = lane >> 5;
    const int ch = ntile * 64 + wn * 32 + l31;
    const unsigned xa0 = wt * 64 + l31, xa1 = xa0 + 32;
    const unsigned ba0 = wn * 64 + l31, ba1 = ba0 + 32;
    const unsigned tq = (lane & 15) >> 2, tp = lane & 3, tblk = (lane >> 4) & 1;

#define K1_FRAGS(F, T, XS, WSM, S)                                                                   \
    do {                                                                                             \
        F[0] = *(const bf16x8*)((XS) + swz128(xa0, 2 * (S) + lh));                                   \
        F[1] = *(const bf16x8*)((XS) + swz128(xa1, 2 * (S) + lh));                                   \
        if (SSM) {                                                                                   \
            F[2] = *(const bf16x8*)((WSM) + swz128(ba0, 2 * (S) + lh));                              \
            F[3] = *(const bf16x8*)((WSM) + swz128(ba1, 2 * (S) + lh));                              \
        } else {                                                                                     \
            const unsigned wb_ = lds_addr_of(WSM);                                                   \
            _Pragma("unroll") for (int i = 0; i < 2; ++i) {                                          \
                unsigned c = 2 * wn + i;                                                             \
                unsigned a0 = off_b(16 * (S) + 8 * lh + 0 + tq, 4 * c + 2 * tblk + (tp >> 1)) + 8 * (tp & 1); \
                unsigned a1 = off_b(16 * (S) + 8 * lh + 4 + tq, 4 * c + 2 * tblk + (tp >> 1)) + 8 * (tp & 1); \
                T[2 * i] = tr_read_asm(wb_ + a0);                                                    \
                T[2 * i + 1] = tr_read_asm(wb_ + a1);                                                \
            }                                                                                        \
        }                                                                                            \
    } while (0)
#define K1_TWAIT(F, T, N)                                                                            \
    do {                                                                                             \
        if (!SSM) {                                                                                  \
            asm volatile("s_waitcnt lgkmcnt(" #N ")" : "+v"(T[0]), "+v"(T[1]), "+v"(T[2]), "+v"(T[3]) :: "memory"); \
            F[2] = __builtin_shufflevector(T[0], T[1], 0, 1, 2, 3, 4, 5, 6, 7);                      \
            F[3] = __builtin_shufflevector(T[2], T[3], 0, 1, 2, 3, 4, 5, 6, 7);                      \
        }                                                                                            \
    } while (0)
#define K1_MMA(F)                                                                                    \
    do {                                                                                             \
        if (SSM) {                                                                                   \
            acc[0][0] = __builtin_amdgcn_mfma_f32_32x32x16_bf16(F[0], F[2], acc[0][0], 0, 0, 0);     \
            acc[0][1] = __builtin_amdgcn_mfma_f32_32x32x16_bf16(F[0], F[3], acc[0][1], 0, 0, 0);     \
            acc[1][0] = __builtin_amdgcn_mfma_f32_32x32x16_bf16(F[1], F[2], acc[1][0], 0, 0, 0);     \
            acc[1][1] = __builtin_amdgcn_mfma_f32_32x32x16_bf16(F[1], F[3], acc[1][1], 0, 0, 0);     \
        } else {                                                                                     \
            acc[0][0] = __builtin_amdgcn_mfma_f32_32x32x16_bf16(F[2], F[0], acc[0][0], 0, 0, 0);     \
            acc[0][1] = __builtin_amdgcn_mfma_f32_32x32x16_bf16(F[2], F[1], acc[0][1], 0, 0, 0);     \
            acc[1][0] = __builtin_amdgcn_mfma_f32_32x32x16_bf16(F[3], F[0], acc[1][0], 0, 0, 0);     \
            acc[1][1] = __builtin_amdgcn_mfma_f32_32x32x16_bf16(F[3], F[1], acc[1][1], 0, 0, 0);     \
        }                                                                                            \
    } while (0)
#define K1_STEP(KT)                                                                                  \
    do {                                                                                             \
        STAMP(0, 8 + 2 * (KT));                                                                      \
        if ((KT) + 1 < 10) asm volatile("s_waitcnt vmcnt(6)" ::: "memory");                          \
        else asm volatile("s_waitcnt vmcnt(0)" ::: "memory");                                        \
        RAW_BARRIER();                                                                               \
        STAMP(0, 9 + 2 * (KT));                                                                      \
        if ((KT) + 2 < 10) K1_GLDS((KT) + 2);                                                        \
        char* xs = smem + ((KT) % 3) * K1_STAGE_BYTES;                                               \
        char* wsm = xs + 32768;                                                                      \
        bf16x8 fA[4], fB[4];                                                                         \
        bf16x4 tA[4], tB[4];                                                                         \
        K1_FRAGS(fA, tA, xs, wsm, 0);                                                                \
        K1_FRAGS(fB, tB, xs, wsm, 1);                                                                \
        __builtin_amdgcn_sched_barrier(0);                                                           \
        K1_TWAIT(fA, tA, 6);                                                                         \
        K1_MMA(fA);                                                                                  \
        __builtin_amdgcn_sched_barrier(0);                                                           \
        K1_FRAGS(fA, tA, xs, wsm, 2);                                                                \
        __builtin_amdgcn_sched_barrier(0);                                                           \
        K1_TWAIT(fB, tB, 6);                                                                         \
        K1_MMA(fB);                                                                                  \
        __builtin_amdgcn_sched_barrier(0);                                                           \
        K1_FRAGS(fB, tB, xs, wsm, 3);                                                                \
        __builtin_amdgcn_sched_barrier(0);                                                           \
        K1_TWAIT(fA, tA, 6);                                                                         \
        K1_MMA(fA);                                                                                  \
        __builtin_amdgcn_sched_barrier(0);                                                           \
        K1_TWAIT(fB, tB, 0);                                                                         \
        K1_MMA(fB);                                                                                  \
        __builtin_amdgcn_sched_barrier(0);                                                           \
    } while (0)
    K1_STEP(0);
    K1_STEP(1);
    K1_STEP(2);
    K1_STEP(3);
    K1_STEP(4);
    K1_STEP(5);
    K1_STEP(6);
    K1_STEP(7);
    K1_STEP(8);
    K1_STEP(9);
#undef K1_STEP
#undef K1_FRAGS
#undef K1_TWAIT
#undef K1_MMA
#undef K1_GLDS
    float Lr = 0.f, Li = 0.f, cr = 0.f, ci = 0.f;
    if (SSM) {
        const unsigned short* cst = (const unsigned short*)(smem + K1_CONST_OFF);
        const int cl = wn * 32 + l31;
        chan_consts(bf2f(cst[cl]), bf2f(cst[128 + cl]), bf2f(cst[256 + cl]), Lr, Li, cr, ci);
    }
    STAMP(0, SSM ? 1 : 2);

    if (SSM) {
        float L2r = CMUL_R(Lr, Li, Lr, Li), L2i = CMUL_I(Lr, Li, Lr, Li);
        float L3r = CMUL_R(L2r, L2i, Lr, Li), L3i = CMUL_I(L2r, L2i, Lr, Li);
        float L4r = CMUL_R(L2r, L2i, L2r, L2i), L4i = CMUL_I(L2r, L2i, L2r, L2i);
        f32x16 vr[2], vi[2];
#pragma unroll
        for (int mi = 0; mi < 2; ++mi)
#pragma unroll
            for (int r = 0; r < 16; ++r) {
                float ar = acc[mi][0][r], ai = acc[mi][1][r];
                vr[mi][r] = cr * ar - ci * ai;
                vi[mi][r] = cr * ai + ci * ar;
            }
#pragma unroll
        for (int mi = 0; mi < 2; ++mi)
#pragma unroll
            for (int j = 0; j < 4; ++j)
#pragma unroll
                for (int i = 1; i < 4; ++i) {
                    int b = 4 * j + i;
                    float pr = vr[mi][b - 1], pi = vi[mi][b - 1];
                    vr[mi][b] += Lr * pr - Li * pi;
                    vi[mi][b] += Lr * pi + Li * pr;
                }
        float cR = 0.f, cI = 0.f;
#pragma unroll
        for (int mi = 0; mi < 2; ++mi)
#pragma unroll
            for (int j = 0; j < 4; ++j) {
                unsigned er = __float_as_uint(vr[mi][4 * j + 3]), ei = __float_as_uint(vi[mi][4 * j + 3]);
                auto sr = __builtin_amdgcn_permlane32_swap(er, er, false, false);
                auto si = __builtin_amdgcn_permlane32_swap(ei, ei, false, false);
                float E0r = __uint_as_float(sr[0]), E0i = __uint_as_float(si[0]);
                float E1r = __uint_as_float(sr[1]), E1i = __uint_as_float(si[1]);
                float c0r = cR, c0i = cI;
                float c1r = L4r * cR - L4i * cI + E0r;
                float c1i = L4r * cI + L4i * cR + E0i;
                cR = L4r * c1r - L4i * c1i + E1r;
                cI = L4r * c1i + L4i * c1r + E1i;
                float inr = lh ? c1r : c0r, ini = lh ? c1i : c0i;
                vr[mi][4 * j + 0] += Lr * inr - Li * ini;
                vi[mi][4 * j + 0] += Lr * ini + Li * inr;
                vr[mi][4 * j + 1] += L2r * inr - L2i * ini;
                vi[mi][4 * j + 1] += L2r * ini + L2i * inr;
                vr[mi][4 * j + 2] += L3r * inr - L3i * ini;
                vi[mi][4 * j + 2] += L3r * ini + L3i * inr;
                vr[mi][4 * j + 3] += L4r * inr - L4i * ini;
                vi[mi][4 * j + 3] += L4r * ini + L4i * inr;
            }
        f32x4* local = (f32x4*)(p.ws + WS_LOCAL);
#pragma unroll
        for (int mi = 0; mi < 2; ++mi)
#pragma unroll
            for (int j = 0; j < 4; ++j) {
                int tq4 = (t0 + wt * 64 + mi * 32 + 8 * j + 4 * lh) >> 2;
                size_t o = ((size_t)tq4 * PN + ch) * 2;
                st_out(&local[o], (f32x4){vr[mi][4 * j], vi[mi][4 * j], vr[mi][4 * j + 1], vi[mi][4 * j + 1]});
                st_out(&local[o + 1], (f32x4){vr[mi][4 * j + 2], vi[mi][4 * j + 2], vr[mi][4 * j + 3], vi[mi][4 * j + 3]});
            }
        if (lh == 0) {
            f32x2* agg = (f32x2*)(p.ws + WS_AGG);
            agg[(size_t)((t0 >> 6) + wt) * PN + ch] = (f32x2){cR, cI};
        }
        STAMP(0, 3);
        STAMP_DRAIN(0, 4);
    } else {
        u32x4* hid = (u32x4*)(p.ws + WS_HIDA);
#pragma unroll
        for (int fi = 0; fi < 2; ++fi) {
            int f0 = ntile * 128 + wn * 64 + fi * 32;
            if (f0 < FF) {
                int ks = f0 >> 5;
#pragma unroll
                for (int mi = 0; mi < 2; ++mi) {
                    int t = t0 + wt * 64 + mi * 32 + l31;
                    int mt = t >> 4;
#pragma unroll
                    for (int half = 0; half < 2; ++half) {
                        u32x4 pk;
#pragma unroll
                        for (int j = 0; j < 4; ++j) {
                            float v0 = acc[fi][mi][8 * half + 2 * j];
                            float v1 = acc[fi][mi][8 * half + 2 * j + 1];
                            v0 = v0 > 0.f ? v0 : 0.f;
                            v1 = v1 > 0.f ? v1 : 0.f;
                            pk[j] = pk2(v0, v1);
                        }
                        int q = 2 * half + lh;
                        st_out(&hid[((size_t)mt * KS_MLP + ks) * 64 + (t & 15) + 16 * q], pk);
                    }
                }
            }
        }
        STAMP_DRAIN(0, 5);
    }
}

__global__ __launch_bounds__(512, 2) void k1_kernel(K1Params p) {
    __shared__ __attribute__((aligned(16))) char smem[3 * K1_STAGE_BYTES + 768];
    const int tid = threadIdx.x;
    const int lane = tid & 63;
    const int wid = tid >> 6;
    int bid = blockIdx.x;

    if (bid < K1_GEMM_BLOCKS) {
        int xcd = bid & 7, idx = bid >> 3;
        int nt24 = xcd * 3 + idx / 8, mtile = idx % 8;
        if (nt24 < 10) k1_tile<true>(p, smem, mtile, nt24);
        else k1_tile<false>(p, smem, mtile, nt24 - 10);
        return;
    }
    bid -= K1_GEMM_BLOCKS;
    if (bid < K1_G_BLOCKS) {
        float* g = (float*)(p.ws + WS_G);
        const int l16 = lane & 15;
        u32x4 xv[2][5], gv[5];
#pragma unroll
        for (int i = 0; i < 5; ++i) gv[i] = *(const u32x4*)(p.gw + (l16 + 16 * i) * 8);
#pragma unroll
        for (int u = 0; u < 2; ++u) {
            const int t = ((bid * 8 + wid) * 2 + u) * 4 + (lane >> 4);
#pragma unroll
            for (int i = 0; i < 5; ++i) xv[u][i] = *(const u32x4*)(p.x + (size_t)t * DM + (l16 + 16 * i) * 8);
        }
#pragma unroll
        for (int u = 0; u < 2; ++u) {
            const int t = ((bid * 8 + wid) * 2 + u) * 4 + (lane >> 4);
            float sum = 0.f;
#pragma unroll
            for (int i = 0; i < 5; ++i)
#pragma unroll
                for (int e = 0; e < 4; ++e) {
                    sum += bfr(bflo(xv[u][i][e]) * bflo(gv[i][e]));
                    sum += bfr(bfhi(xv[u][i][e]) * bfhi(gv[i][e]));
                }
#pragma unroll
            for (int o = 8; o > 0; o >>= 1) sum += __shfl_xor(sum, o);
            if (l16 == 0) {
                float z = bfr(sum);
                g[t] = bfr(1.0f / (1.0f + expf(-z)));
            }
        }
        return;
    }
    bid -= K1_G_BLOCKS;
    if (bid < K1_W2_BLOCKS) {
        u32x4* w2t = (u32x4*)(p.ws + WS_W2T);
        char* img = smem + wid * 8192;
        u32x4 v[2][4];
#pragma unroll
        for (int u = 0; u < 2; ++u) {
            int task = (bid * 8 + wid) * 2 + u;
            if (task > KS_MLP * 10 - 1) task = KS_MLP * 10 - 1;
            int ks = task / 10, ng = task % 10;
#pragma unroll
            for (int i = 0; i < 4; ++i) {
                int idx = lane + 64 * i, row = idx >> 3, c8 = idx & 7;
                v[u][i] = *(const u32x4*)(p.w2 + (size_t)(32 * ks + row) * DM + ng * 64 + c8 * 8);
            }
        }
#pragma unroll
        for (int u = 0; u < 2; ++u)
#pragma unroll
            for (int i = 0; i < 4; ++i) {
                int idx = lane + 64 * i, row = idx >> 3, c8 = idx & 7;
                *(u32x4*)(img + u * 4096 + row * 128 + c8 * 16) = v[u][i];
            }
        __builtin_amdgcn_fence(__ATOMIC_RELEASE, "wavefront");
        __builtin_amdgcn_wave_barrier();
        __builtin_amdgcn_fence(__ATOMIC_ACQUIRE, "wavefront");
        const int q = lane >> 4, qq = (lane & 15) >> 2, pp = lane & 3;
#pragma unroll
        for (int u = 0; u < 2; ++u) {
            int task = (bid * 8 + wid) * 2 + u;
            if (task < KS_MLP * 10) {
                int ks = task / 10, ng = task % 10;
#pragma unroll
                for (int c = 0; c < 4; ++c) {
                    int r0 = 16 * (q >> 1) + 4 * (q & 1);
                    unsigned a0 = u * 4096 + (r0 + qq) * 128 + c * 32 + pp * 8;
                    unsigned a1 = a0 + 8 * 128;
                    bf16x4 lo = __builtin_amdgcn_ds_read_tr16_b64_v4bf16((lds_bf16x4_ptr)(img + a0));
                    bf16x4 hi = __builtin_amdgcn_ds_read_tr16_b64_v4bf16((lds_bf16x4_ptr)(img + a1));
                    bf16x8 f = __builtin_shufflevector(lo, hi, 0, 1, 2, 3, 4, 5, 6, 7);
                    w2t[((size_t)(ng * 4 + c) * KS_MLP + ks) * 64 + lane] = __builtin_bit_cast(u32x4, f);
                }
            }
        }
        return;
    }
    bid -= K1_W2_BLOCKS;
    {
        u32x4* ct = (u32x4*)(p.ws + WS_CT);
        u32x4 re[4], im[4];
#pragma unroll
        for (int u = 0; u < 4; ++u) {
            int task = (bid * 8 + wid) * 4 + u;
            int nt = task / 20, pg = task % 20;
            int d = nt * 16 + (lane & 15), pc = pg * 32 + (lane >> 4) * 8;
            re[u] = *(const u32x4*)(p.cre + (size_t)d * PN + pc);
            im[u] = *(const u32x4*)(p.cim + (size_t)d * PN + pc);
        }
#pragma unroll
        for (int u = 0; u < 4; ++u) {
            int task = (bid * 8 + wid) * 4 + u;
            int nt = task / 20, pg = task % 20;
            int c = lane >> 4;
            int ks = pg * 2 + (c >> 1), q0 = 2 * (c & 1);
            u32x4 pa, pb;
            pa[0] = pk2(2.0f * bflo(re[u][0]), -2.0f * bflo(im[u][0]));
            pa[1] = pk2(2.0f * bfhi(re[u][0]), -2.0f * bfhi(im[u][0]));
            pa[2] = pk2(2.0f * bflo(re[u][1]), -2.0f * bflo(im[u][1]));
            pa[3] = pk2(2.0f * bfhi(re[u][1]), -2.0f * bfhi(im[u][1]));
            pb[0] = pk2(2.0f * bflo(re[u][2]), -2.0f * bflo(im[u][2]));
            pb[1] = pk2(2.0f * bfhi(re[u][2]), -2.0f * bfhi(im[u][2]));
            pb[2] = pk2(2.0f * bflo(re[u][3]), -2.0f * bflo(im[u][3]));
            pb[3] = pk2(2.0f * bfhi(re[u][3]), -2.0f * bfhi(im[u][3]));
            size_t o = ((size_t)nt * KS_SSM + ks) * 64 + (lane & 15);
            ct[o + 16 * q0] = pa;
            ct[o + 16 * (q0 + 1)] = pb;
        }
    }
}

struct Frag9 { u32x4 a[4]; u32x4 b[5]; };

template <int KS>
__device__ __forceinline__ void g2_load(Frag9& f, const u32x4* __restrict__ ap, const u32x4* __restrict__ bp, int ks) {
#pragma unroll
    for (int i = 0; i < 4; ++i) f.a[i] = ap[((size_t)i * KS + ks) * 64];
#pragma unroll
    for (int j = 0; j < 5; ++j) f.b[j] = bp[((size_t)j * KS + ks) * 64];
    __builtin_amdgcn_sched_barrier(0);
}
__device__ __forceinline__ void g2_mma(const Frag9& f, f32x4 (&acc)[4][5]) {
#pragma unroll
    for (int i = 0; i < 4; ++i)
#pragma unroll
        for (int j = 0; j < 5; ++j)
            acc[i][j] = __builtin_amdgcn_mfma_f32_16x16x32_bf16(__builtin_bit_cast(bf16x8, f.b[j]),
                                                                __builtin_bit_cast(bf16x8, f.a[i]), acc[i][j], 0, 0, 0);
    __builtin_amdgcn_sched_barrier(0);
}

template <int KS, int NW, int MODE>
__device__ __forceinline__ void gemm2_body(const u32x4* __restrict__ A, const u32x4* __restrict__ B,
                                           int mb, int nb, float* red, const float* __restrict__ g,
                                           const unsigned short* __restrict__ x,
                                           const unsigned short* __restrict__ dskip, float* __restrict__ out,
                                           char* stamp_base, int kid) {
    STAMP(kid, 0);
    const int lane = threadIdx.x & 63;
    const int wid = __builtin_amdgcn_readfirstlane(threadIdx.x >> 6);
    constexpr int NS = (KS + NW - 1) / NW;
    f32x4 acc[4][5];
#pragma unroll
    for (int i = 0; i < 4; ++i)
#pragma unroll
        for (int j = 0; j < 5; ++j) acc[i][j] = (f32x4){0.f, 0.f, 0.f, 0.f};
    const u32x4* ap = A + ((size_t)(mb * 4) * KS) * 64 + lane;
    const u32x4* bp = B + ((size_t)(nb * 5) * KS) * 64 + lane;
    Frag9 f0, f1, f2, f3;
    g2_load<KS>(f0, ap, bp, wid);
    g2_load<KS>(f1, ap, bp, wid + NW);
    g2_load<KS>(f2, ap, bp, wid + 2 * NW);
    g2_load<KS>(f3, ap, bp, wid + 3 * NW);
    STAMP(kid, 1);
#pragma unroll
    for (int s = 0; s < NS; ++s) {
        const int kn = wid + NW * (s + 4);
        const bool last_ragged = (s == NS - 1) && (KS % NW != 0);
        const bool do_mma = !last_ragged || (wid + NW * s < KS);
        if ((s & 3) == 0) { if (do_mma) g2_mma(f0, acc); if (s + 4 < NS) g2_load<KS>(f0, ap, bp, kn < KS ? kn : KS - 1); }
        if ((s & 3) == 1) { if (do_mma) g2_mma(f1, acc); if (s + 4 < NS) g2_load<KS>(f1, ap, bp, kn < KS ? kn : KS - 1); }
        if ((s & 3) == 2) { if (do_mma) g2_mma(f2, acc); if (s + 4 < NS) g2_load<KS>(f2, ap, bp, kn < KS ? kn : KS - 1); }
        if ((s & 3) == 3) { if (do_mma) g2_mma(f3, acc); if (s + 4 < NS) g2_load<KS>(f3, ap, bp, kn < KS ? kn : KS - 1); }
    }
    STAMP(kid, 2);
    WSTAMP(kid, 0);
    constexpr int NOWN = (20 + NW - 1) / NW;
    f32x4 eo[NOWN];
    u32x2 ex[NOWN], ed[NOWN];
    float eg[NOWN];
#pragma unroll
    for (int o = 0; o < NOWN; ++o) {
        int tau = wid + NW * o;
        if (tau > 19) tau = 19;
        int i = tau / 5, j = tau % 5;
        int t = mb * 64 + i * 16 + (lane & 15);
        int d = nb * 80 + j * 16 + (lane >> 4) * 4;
        eg[o] = g[t];
        if (MODE == 1) {
            eo[o] = *(const f32x4*)(out + (size_t)t * DM + d);
            ex[o] = *(const u32x2*)(x + (size_t)t * DM + d);
            ed[o] = *(const u32x2*)(dskip + d);
        }
    }
    f32x4* r4 = (f32x4*)red;
#pragma unroll
    for (int tau = 0; tau < 20; ++tau) {
        const int owner = tau % NW;
        if (wid != owner) {
            int wslot = wid < owner ? wid : wid - 1;
            r4[(tau * (NW - 1) + wslot) * 64 + lane] = acc[tau / 5][tau % 5];
        }
    }
    WSTAMP(kid, 1);
    __syncthreads();
    WSTAMP(kid, 2);
#pragma unroll
    for (int tau = 0; tau < 20; ++tau) {
        if (wid == tau % NW) {
            const int o = tau / NW;
            f32x4 sacc = acc[tau / 5][tau % 5];
#pragma unroll
            for (int w = 0; w < NW - 1; ++w) sacc += r4[(tau * (NW - 1) + w) * 64 + lane];
            const int i = tau / 5, j = tau % 5;
            int t = mb * 64 + i * 16 + (lane & 15);
            int d = nb * 80 + j * 16 + (lane >> 4) * 4;
            f32x4 res;
            if (MODE == 0) {
                float omg = bfr(1.0f - eg[o]);
#pragma unroll
                for (int r = 0; r < 4; ++r) res[r] = bfr(omg * bfr(sacc[r]));
            } else {
                float s0 = bfr(bflo(ex[o][0]) * bflo(ed[o][0]));
                float s1 = bfr(bfhi(ex[o][0]) * bfhi(ed[o][0]));
                float s2 = bfr(bflo(ex[o][1]) * bflo(ed[o][1]));
                float s3 = bfr(bfhi(ex[o][1]) * bfhi(ed[o][1]));
                res[0] = eg[o] * (sacc[0] + s0) + eo[o][0];
                res[1] = eg[o] * (sacc[1] + s1) + eo[o][1];
                res[2] = eg[o] * (sacc[2] + s2) + eo[o][2];
                res[3] = eg[o] * (sacc[3] + s3) + eo[o][3];
            }
            st_out((f32x4*)(out + (size_t)t * DM + d), res);
        }
    }
    WSTAMP(kid, 3);
    STAMP(kid, 3);
    STAMP_DRAIN(kid, 4);
}

#define K2_FIX_BLOCKS (NCHUNK * 5)
#define K2_GEMM_BLOCKS 256
struct K2Params {
    const unsigned short* lre;
    const unsigned short* lim;
    const unsigned short* ldt;
    char* ws;
    float* out;
};

__global__ __launch_bounds__(256, 2) void k2_kernel(K2Params p) {
    __shared__ __attribute__((aligned(16))) float smem[16384];
    const int tid = threadIdx.x, lane = tid & 63, wid = tid >> 6;
    int bid = blockIdx.x;
    if (bid >= K2_FIX_BLOCKS) {
        bid -= K2_FIX_BLOCKS;
        int xcd = bid & 7, idx = bid >> 3;
        int mb = xcd * 4 + (idx >> 3), nb = idx & 7;
        gemm2_body<KS_MLP, 4, 0>((const u32x4*)(p.ws + WS_HIDA), (const u32x4*)(p.ws + WS_W2T), mb, nb, smem,
                                 (const float*)(p.ws + WS_G), nullptr, nullptr, p.out, p.ws, 1);
        return;
    }
    {
        const int c = bid / 5, cg = bid % 5;
        f32x2* corr = (f32x2*)smem;
        const f32x4* local = (const f32x4*)(p.ws + WS_LOCAL);
        f32x4 lv[2][8];
#pragma unroll
        for (int u = 0; u < 2; ++u) {
            int uid = tid + 256 * u;
            int cq = uid & 31, tq4 = uid >> 5;
            size_t o = ((size_t)(c * 16 + tq4) * PN + cg * 128 + cq * 4) * 2;
#pragma unroll
            for (int e = 0; e < 8; ++e) lv[u][e] = local[o + e];
        }
        {
            const int chl = tid & 127, part = tid >> 7;
            const int ch = cg * 128 + chl;
            const f32x2* agg = (const f32x2*)(p.ws + WS_AGG);
            float Lr, Li;
            unsigned short a0 = p.lre[ch], a1 = p.lim[ch], a2 = p.ldt[ch];
            f32x2 e[NCHUNK - 1];
#pragma unroll
            for (int cc = 0; cc < NCHUNK - 1; ++cc) {
                int ci2 = cc < c ? cc : 0;
                e[cc] = agg[(size_t)ci2 * PN + ch];
            }
            chan_lam(bf2f(a0), bf2f(a1), bf2f(a2), Lr, Li);
            float Pr = Lr, Pi = Li;
#pragma unroll
            for (int i = 0; i < 5; ++i) {
                float nr = Pr * Pr - Pi * Pi, ni = 2.f * Pr * Pi;
                Pr = nr; Pi = ni;
            }
            const float Qr = Pr, Qi = Pi;
            Pr = Qr * Qr - Qi * Qi; Pi = 2.f * Qr * Qi;
            float Hr = 0.f, Hi = 0.f;
#pragma unroll
            for (int cc = 0; cc < NCHUNK - 1; ++cc) {
                if (cc < c) {
                    float nr = Pr * Hr - Pi * Hi + e[cc][0];
                    float ni = Pr * Hi + Pi * Hr + e[cc][1];
                    Hr = nr; Hi = ni;
                }
            }
            if (part) { float nr = Qr * Hr - Qi * Hi, ni = Qr * Hi + Qi * Hr; Hr = nr; Hi = ni; }
#pragma unroll 8
            for (int n = 0; n < 32; ++n) {
                float nr = Lr * Hr - Li * Hi, ni = Lr * Hi + Li * Hr;
                Hr = nr; Hi = ni;
                corr[(part * 32 + n) * 128 + chl] = (f32x2){Hr, Hi};
            }
        }
        __syncthreads();
        u32x4* ha = (u32x4*)(p.ws + WS_HA);
#pragma unroll
        for (int u = 0; u < 2; ++u) {
            int uid = tid + 256 * u;
            int cq = uid & 31, tq4 = uid >> 5;
            int chl = cq * 4;
#pragma unroll
            for (int tt = 0; tt < 4; ++tt) {
                int tl = tq4 * 4 + tt;
                f32x4 c0 = *(const f32x4*)&corr[tl * 128 + chl];
                f32x4 c1 = *(const f32x4*)&corr[tl * 128 + chl + 2];
                u32x4 pk;
                pk[0] = pk2(lv[u][0 + (tt >> 1)][2 * (tt & 1)] + c0[0], lv[u][0 + (tt >> 1)][2 * (tt & 1) + 1] + c0[1]);
                pk[1] = pk2(lv[u][2 + (tt >> 1)][2 * (tt & 1)] + c0[2], lv[u][2 + (tt >> 1)][2 * (tt & 1) + 1] + c0[3]);
                pk[2] = pk2(lv[u][4 + (tt >> 1)][2 * (tt & 1)] + c1[0], lv[u][4 + (tt >> 1)][2 * (tt & 1) + 1] + c1[1]);
                pk[3] = pk2(lv[u][6 + (tt >> 1)][2 * (tt & 1)] + c1[2], lv[u][6 + (tt >> 1)][2 * (tt & 1) + 1] + c1[3]);
                int t = c * 64 + tl;
                int ks = cg * 8 + (cq >> 2), q = cq & 3, mt = t >> 4;
                st_out(&ha[((size_t)mt * KS_SSM + ks) * 64 + (t & 15) + 16 * q], pk);
            }
        }
    }
}

struct K3Params {
    const unsigned short* x;
    const unsigned short* dskip;
    char* ws;
    float* out;
};

__global__ __launch_bounds__(256, 2) void k3_kernel(K3Params p) {
    __shared__ __attribute__((aligned(16))) float smem[20 * 3 * 256];
    int bid = blockIdx.x;
    int xcd = bid & 7, idx = bid >> 3;
    int mb = xcd * 4 + (idx >> 3), nb = idx & 7;
    gemm2_body<KS_SSM, 4, 1>((const u32x4*)(p.ws + WS_HA), (const u32x4*)(p.ws + WS_CT), mb, nb, smem,
                             (const float*)(p.ws + WS_G), p.x, p.dskip, p.out, p.ws, 2);
}

extern "C" void kernel_launch(void* const* d_in, const int* in_sizes, int n_in, void* d_out, int out_size,
                              void* d_ws, size_t ws_size, hipStream_t stream) {
    const unsigned short* x = (const unsigned short*)d_in[0];
    const unsigned short* lre = (const unsigned short*)d_in[1];
    const unsigned short* lim = (const unsigned short*)d_in[2];
    const unsigned short* bre = (const unsigned short*)d_in[3];
    const unsigned short* bim = (const unsigned short*)d_in[4];
    const unsigned short* cre = (const unsigned short*)d_in[5];
    const unsigned short* cim = (const unsigned short*)d_in[6];
    const unsigned short* ldt = (const unsigned short*)d_in[7];
    const unsigned short* dskip = (const unsigned short*)d_in[8];
    const unsigned short* w1 = (const unsigned short*)d_in[10];
    const unsigned short* w2 = (const unsigned short*)d_in[11];
    const unsigned short* gw = (const unsigned short*)d_in[12];
    char* ws = (char*)d_ws;
    float* out = (float*)d_out;

    K1Params p1{x, lre, lim, bre, bim, cre, cim, ldt, w1, w2, gw, ws};
    k1_kernel<<<K1_GRID, 512, 0, stream>>>(p1);
    K2Params p2{lre, lim, ldt, ws, out};
    k2_kernel<<<K2_GEMM_BLOCKS + K2_FIX_BLOCKS, 256, 0, stream>>>(p2);
    K3Params p3{x, dskip, ws, out};
    k3_kernel<<<256, 256, 0, stream>>>(p3);
}
```

```cpp
#include <hip/hip_runtime.h>
#include <hip/hip_bf16.h>
#include <stdint.h>

typedef __attribute__((ext_vector_type(8))) __bf16 bf16x8;
typedef __attribute__((ext_vector_type(4))) __bf16 bf16x4;
typedef __attribute__((ext_vector_type(16))) float f32x16;
typedef __attribute__((ext_vector_type(4))) float f32x4;
typedef __attribute__((ext_vector_type(2))) float f32x2;
typedef __attribute__((ext_vector_type(4))) unsigned u32x4;
typedef __attribute__((ext_vector_type(2))) unsigned u32x2;
typedef __attribute__((address_space(3))) bf16x4* lds_bf16x4_ptr;

#define LSEQ 2048
#define DM 640
#define PN 640
#define FF 1728
#define CHUNK 64
#define NCHUNK (LSEQ / CHUNK)
#define KS_MLP (FF / 32)
#define KS_SSM (2 * PN / 32)
#define MT (LSEQ / 16)
#define NT (DM / 16)

#define WS_LOCAL 0
#define WS_LOCAL_SZ ((size_t)LSEQ * PN * 8)
#define WS_AGG (WS_LOCAL + WS_LOCAL_SZ)
#define WS_AGG_SZ ((size_t)NCHUNK * PN * 8)
#define WS_HIDA (WS_AGG + WS_AGG_SZ)
#define WS_HIDA_SZ ((size_t)MT * KS_MLP * 1024)
#define WS_HA (WS_HIDA + WS_HIDA_SZ)
#define WS_HA_SZ ((size_t)MT * KS_SSM * 1024)
#define WS_W2T (WS_HA + WS_HA_SZ)
#define WS_W2T_SZ ((size_t)NT * KS_MLP * 1024)
#define WS_CT (WS_W2T + WS_W2T_SZ)
#define WS_CT_SZ ((size_t)NT * KS_SSM * 1024)
#define WS_G (WS_CT + WS_CT_SZ)

#define STAMP(kid, idx) do {} while (0)
#define STAMP_DRAIN(kid, idx) do {} while (0)
#define WSTAMP(kid, idx) do {} while (0)

__device__ __forceinline__ float bf2f(unsigned short u) { return __uint_as_float(((unsigned)u) << 16); }
__device__ __forceinline__ float bflo(unsigned u) { return __uint_as_float(u << 16); }
__device__ __forceinline__ float bfhi(unsigned u) { return __uint_as_float(u & 0xffff0000u); }
__device__ __forceinline__ float bfr(float x) { return (float)(__bf16)x; }
__device__ __forceinline__ unsigned short f2bf(float x) {
    __bf16 b = (__bf16)x;
    return __builtin_bit_cast(unsigned short, b);
}
#define NT_STORES 0
template <typename T>
__device__ __forceinline__ void st_out(T* ptr, T v) {
    *ptr = v;
}
__device__ __forceinline__ unsigned pk2(float a, float b) { return f2bf(a) | ((unsigned)f2bf(b) << 16); }

__device__ __forceinline__ void sincos_acc(float x, float& s, float& c) {
    float kf = rintf(x * 0.63661977236758134f);
    float r = fmaf(kf, -1.5707962513e+00f, x);
    r = fmaf(kf, -7.5497894159e-08f, r);
    r = fmaf(kf, -5.3903029534e-15f, r);
    int k = (int)kf;
    float r2 = r * r;
    float sp = fmaf(r2, -1.9515295891e-04f, 8.3321608736e-03f);
    sp = fmaf(sp, r2, -1.6666654611e-01f);
    sp = fmaf(sp * r2, r, r);
    float cp = fmaf(r2, 2.443315711809948e-05f, -1.388731625493765e-03f);
    cp = fmaf(cp, r2, 4.166664568298827e-02f);
    cp = fmaf(cp * r2, r2, fmaf(-0.5f, r2, 1.0f));
    float ss = (k & 1) ? cp : sp;
    float cc = (k & 1) ? sp : cp;
    s = (k & 2) ? -ss : ss;
    c = ((k + 1) & 2) ? -cc : cc;
}

__device__ __forceinline__ void chan_lam(float lre, float lim, float ldt, float& Lr, float& Li) {
    float dt = bfr(expf(ldt));
    float a = dt * lre, b = dt * lim;
    float ea = expf(a);
    float sb, cb;
    sincos_acc(b, sb, cb);
    Lr = ea * cb;
    Li = ea * sb;
}
__device__ __forceinline__ void chan_consts(float lre, float lim, float ldt, float& Lr, float& Li,
                                            float& cr, float& ci) {
    float dt = bfr(expf(ldt));
    float a = dt * lre, b = dt * lim;
    float ea = expf(a);
    float sb, cb;
    sincos_acc(b, sb, cb);
    Lr = ea * cb;
    Li = ea * sb;
    float sh, chh;
    sincos_acc(0.5f * b, sh, chh);
    float nr = expm1f(a) * cb - 2.0f * sh * sh;
    float ni = Li;
    float den = lre * lre + lim * lim;
    float inv = 1.0f / den;
    cr = (nr * lre + ni * lim) * inv;
    ci = (ni * lre - nr * lim) * inv;
}

#define CMUL_R(ar, ai, br, bi) ((ar) * (br) - (ai) * (bi))
#define CMUL_I(ar, ai, br, bi) ((ar) * (bi) + (ai) * (br))

#define K1_GEMM_BLOCKS 192
#define K1_G_BLOCKS 32
#define K1_W2_BLOCKS 34
#define K1_C_BLOCKS 25
#define K1_GRID (K1_GEMM_BLOCKS + K1_G_BLOCKS + K1_W2_BLOCKS + K1_C_BLOCKS)

__device__ __forceinline__ unsigned swz128(unsigned row, unsigned ch) {
    return row * 128u + ((ch ^ ((row >> 1) & 7u)) << 4);
}
__device__ __forceinline__ unsigned off_b(unsigned row, unsigned ch) {
    return 256u * row + 16u * (ch ^ (((row & 3u) << 2) | ((row >> 2) & 3u)));
}

struct K1Params {
    const unsigned short* x;
    const unsigned short* lre;
    const unsigned short* lim;
    const unsigned short* bre;
    const unsigned short* bim;
    const unsigned short* cre;
    const unsigned short* cim;
    const unsigned short* ldt;
    const unsigned short* w1;
    const unsigned short* w2;
    const unsigned short* gw;
    char* ws;
};

__device__ __forceinline__ bf16x4 tr_read_asm(unsigned lds_addr) {
    bf16x4 r;
    asm volatile("ds_read_b64_tr_b16 %0, %1" : "=v"(r) : "v"(lds_addr) : "memory");
    return r;
}
__device__ __forceinline__ bf16x8 lds_read128_asm(unsigned lds_addr) {
    bf16x8 r;
    asm volatile("ds_read_b128 %0, %1" : "=v"(r) : "v"(lds_addr) : "memory");
    return r;
}
__device__ __forceinline__ unsigned lds_addr_of(const char* ptr) {
    return (unsigned)(size_t)(__attribute__((address_space(3))) const char*)ptr;
}
#define K1_STAGE_BYTES 49152
#define K1_CONST_OFF (3 * K1_STAGE_BYTES)
#define RAW_BARRIER() do { asm volatile("s_waitcnt lgkmcnt(0)" ::: "memory"); __builtin_amdgcn_s_barrier(); } while (0)

template <bool SSM>
__device__ __forceinline__ void k1_tile(const K1Params& p, char* smem, int mtile, int ntile) {
    const int tid = threadIdx.x;
    const int lane = tid & 63;
    const int wid = __builtin_amdgcn_readfirstlane(tid >> 6);
    const int t0 = mtile * 256;
    const int wt = wid >> 1, wn = wid & 1;
    char* stamp_base = p.ws;
    STAMP(0, 0);

    const unsigned short* xs_src[4];
#pragma unroll
    for (int i = 0; i < 4; ++i) {
        unsigned row = (4 * wid + i) * 8 + (lane >> 3);
        unsigned chk = (lane & 7) ^ ((row >> 1) & 7);
        xs_src[i] = p.x + (size_t)(t0 + row) * DM + chk * 8;
    }
    const unsigned short* ws_src[2];
#pragma unroll
    for (int i = 0; i < 2; ++i) {
        if (SSM) {
            unsigned row = (2 * wid + i) * 8 + (lane >> 3);
            unsigned chk = (lane & 7) ^ ((row >> 1) & 7);
            unsigned grp = row >> 5;
            const unsigned short* base = (grp & 1) ? p.bim : p.bre;
            ws_src[i] = base + (size_t)(ntile * 64 + (row & 31) + 32 * (grp >> 1)) * DM + chk * 8;
        } else {
            unsigned row = (2 * wid + i) * 4 + (lane >> 4);
            unsigned chk = (lane & 15) ^ (((row & 3u) << 2) | ((row >> 2) & 3u));
            int n = ntile * 128 + chk * 8;
            if (n > FF - 8) n = FF - 8;
            ws_src[i] = p.w1 + (size_t)row * FF + n;
        }
    }
#define K1_GLDS(KT)                                                                                  \
    do {                                                                                             \
        char* st_ = smem + ((KT) % 3) * K1_STAGE_BYTES;                                              \
        _Pragma("unroll") for (int i = 0; i < 4; ++i)                                                \
            __builtin_amdgcn_global_load_lds((const unsigned*)(xs_src[i] + (KT) * 64),               \
                                             (unsigned*)(st_ + (4 * wid + i) * 1024), 16, 0, 0);     \
        _Pragma("unroll") for (int i = 0; i < 2; ++i)                                                \
            __builtin_amdgcn_global_load_lds((const unsigned*)(ws_src[i] + (SSM ? (size_t)(KT) * 64 : (size_t)(KT) * 64 * FF)), \
                                             (unsigned*)(st_ + 32768 + (2 * wid + i) * 1024), 16, 0, 0); \
    } while (0)

    if (SSM) {
        const int cc = ntile * 64 + 2 * (lane & 31);
        __builtin_amdgcn_global_load_lds((const unsigned*)(p.lre + cc), (unsigned*)(smem + K1_CONST_OFF), 4, 0, 0);
        __builtin_amdgcn_global_load_lds((const unsigned*)(p.lim + cc), (unsigned*)(smem + K1_CONST_OFF + 256), 4, 0, 0);
        __builtin_amdgcn_global_load_lds((const unsigned*)(p.ldt + cc), (unsigned*)(smem + K1_CONST_OFF + 512), 4, 0, 0);
    }
    K1_GLDS(0);
    K1_GLDS(1);
    K1_GLDS(2);

    f32x16 acc[2][2];
#pragma unroll
    for (int i = 0; i < 2; ++i)
#pragma unroll
        for (int j = 0; j < 2; ++j)
#pragma unroll
            for (int r = 0; r < 16; ++r) acc[i][j][r] = 0.f;

    const unsigned l31 = lane & 31, lh = lane >> 5;
    const int ch = ntile * 64 + wn * 32 + l31;
    const unsigned xa0 = wt * 64 + l31, xa1 = xa0 + 32;
    const unsigned ba0 = wn * 64 + l31, ba1 = ba0 + 32;
    const unsigned tq = (lane & 15) >> 2, tp = lane & 3, tblk = (lane >> 4) & 1;

#define K1_FRAGS(F, T, XS, WSM, S)                                                                   \
    do {                                                                                             \
        const unsigned xb_ = lds_addr_of(XS);                                                        \
        const unsigned wb_ = lds_addr_of(WSM);                                                       \
        F[0] = lds_read128_asm(xb_ + swz128(xa0, 2 * (S) + lh));                                     \
        F[1] = lds_read128_asm(xb_ + swz128(xa1, 2 * (S) + lh));                                     \
        if (SSM) {                                                                                   \
            F[2] = lds_read128_asm(wb_ + swz128(ba0, 2 * (S) + lh));                                 \
            F[3] = lds_read128_asm(wb_ + swz128(ba1, 2 * (S) + lh));                                 \
        } else {                                                                                     \
            _Pragma("unroll") for (int i = 0; i < 2; ++i) {                                          \
                unsigned c = 2 * wn + i;                                                             \
                unsigned a0 = off_b(16 * (S) + 8 * lh + 0 + tq, 4 * c + 2 * tblk + (tp >> 1)) + 8 * (tp & 1); \
                unsigned a1 = off_b(16 * (S) + 8 * lh + 4 + tq, 4 * c + 2 * tblk + (tp >> 1)) + 8 * (tp & 1); \
                T[2 * i] = tr_read_asm(wb_ + a0);                                                    \
                T[2 * i + 1] = tr_read_asm(wb_ + a1);                                                \
            }                                                                                        \
        }                                                                                            \
    } while (0)
#define K1_TWAIT(F, T, LAST)                                                                         \
    do {                                                                                             \
        if (SSM) {                                                                                   \
            if (LAST) asm volatile("s_waitcnt lgkmcnt(0)" : "+v"(F[0]), "+v"(F[1]), "+v"(F[2]), "+v"(F[3]) :: "memory"); \
            else asm volatile("s_waitcnt lgkmcnt(4)" : "+v"(F[0]), "+v"(F[1]), "+v"(F[2]), "+v"(F[3]) :: "memory"); \
        } else {                                                                                     \
            if (LAST) asm volatile("s_waitcnt lgkmcnt(0)" : "+v"(F[0]), "+v"(F[1]), "+v"(T[0]), "+v"(T[1]), "+v"(T[2]), "+v"(T[3]) :: "memory"); \
            else asm volatile("s_waitcnt lgkmcnt(6)" : "+v"(F[0]), "+v"(F[1]), "+v"(T[0]), "+v"(T[1]), "+v"(T[2]), "+v"(T[3]) :: "memory"); \
            F[2] = __builtin_shufflevector(T[0], T[1], 0, 1, 2, 3, 4, 5, 6, 7);                      \
            F[3] = __builtin_shufflevector(T[2], T[3], 0, 1, 2, 3, 4, 5, 6, 7);                      \
        }                                                                                            \
    } while (0)
#define K1_MMA(F)                                                                                    \
    do {                                                                                             \
        if (SSM) {                                                                                   \
            acc[0][0] = __builtin_amdgcn_mfma_f32_32x32x16_bf16(F[0], F[2], acc[0][0], 0, 0, 0);     \
            acc[0][1] = __builtin_amdgcn_mfma_f32_32x32x16_bf16(F[0], F[3], acc[0][1], 0, 0, 0);     \
            acc[1][0] = __builtin_amdgcn_mfma_f32_32x32x16_bf16(F[1], F[2], acc[1][0], 0, 0, 0);     \
            acc[1][1] = __builtin_amdgcn_mfma_f32_32x32x16_bf16(F[1], F[3], acc[1][1], 0, 0, 0);     \
        } else {                                                                                     \
            acc[0][0] = __builtin_amdgcn_mfma_f32_32x32x16_bf16(F[2], F[0], acc[0][0], 0, 0, 0);     \
            acc[0][1] = __builtin_amdgcn_mfma_f32_32x32x16_bf16(F[2], F[1], acc[0][1], 0, 0, 0);     \
            acc[1][0] = __builtin_amdgcn_mfma_f32_32x32x16_bf16(F[3], F[0], acc[1][0], 0, 0, 0);     \
            acc[1][1] = __builtin_amdgcn_mfma_f32_32x32x16_bf16(F[3], F[1], acc[1][1], 0, 0, 0);     \
        }                                                                                            \
    } while (0)
    bf16x8 fA[4], fB[4];
    bf16x4 tA[4], tB[4];
#define K1_STEP(KT)                                                                                  \
    do {                                                                                             \
        char* xs = smem + ((KT) % 3) * K1_STAGE_BYTES;                                               \
        char* wsm = xs + 32768;                                                                      \
        char* xn = smem + (((KT) + 1) % 3) * K1_STAGE_BYTES;                                         \
        char* wn_ = xn + 32768;                                                                      \
        K1_TWAIT(fA, tA, 0);                                                                         \
        K1_MMA(fA);                                                                                  \
        __builtin_amdgcn_sched_barrier(0);                                                           \
        K1_FRAGS(fA, tA, xs, wsm, 2);                                                                \
        __builtin_amdgcn_sched_barrier(0);                                                           \
        K1_TWAIT(fB, tB, 0);                                                                         \
        K1_MMA(fB);                                                                                  \
        __builtin_amdgcn_sched_barrier(0);                                                           \
        K1_FRAGS(fB, tB, xs, wsm, 3);                                                                \
        __builtin_amdgcn_sched_barrier(0);                                                           \
        K1_TWAIT(fA, tA, 0);                                                                         \
        K1_MMA(fA);                                                                                  \
        __builtin_amdgcn_sched_barrier(0);                                                           \
        if ((KT) + 1 < 10) {                                                                         \
            if ((KT) + 2 < 10) asm volatile("s_waitcnt vmcnt(6)" ::: "memory");                      \
            else asm volatile("s_waitcnt vmcnt(0)" ::: "memory");                                    \
            RAW_BARRIER();                                                                           \
            if ((KT) + 3 < 10) K1_GLDS((KT) + 3);                                                    \
            K1_FRAGS(fA, tA, xn, wn_, 0);                                                            \
            __builtin_amdgcn_sched_barrier(0);                                                       \
            K1_TWAIT(fB, tB, 0);                                                                     \
        } else {                                                                                     \
            K1_TWAIT(fB, tB, 1);                                                                     \
        }                                                                                            \
        K1_MMA(fB);                                                                                  \
        __builtin_amdgcn_sched_barrier(0);                                                           \
        if ((KT) + 1 < 10) K1_FRAGS(fB, tB, xn, wn_, 1);                                             \
        __builtin_amdgcn_sched_barrier(0);                                                           \
        STAMP(0, 8 + (KT));                                                                          \
    } while (0)
    asm volatile("s_waitcnt vmcnt(12)" ::: "memory");
    RAW_BARRIER();
    K1_FRAGS(fA, tA, smem, smem + 32768, 0);
    K1_FRAGS(fB, tB, smem, smem + 32768, 1);
    __builtin_amdgcn_sched_barrier(0);
    K1_STEP(0);
    K1_STEP(1);
    K1_STEP(2);
    K1_STEP(3);
    K1_STEP(4);
    K1_STEP(5);
    K1_STEP(6);
    K1_STEP(7);
    K1_STEP(8);
    K1_STEP(9);
#undef K1_STEP
#undef K1_FRAGS
#undef K1_TWAIT
#undef K1_MMA
#undef K1_GLDS
    float Lr = 0.f, Li = 0.f, cr = 0.f, ci = 0.f;
    if (SSM) {
        const unsigned short* cst = (const unsigned short*)(smem + K1_CONST_OFF);
        const int cl = wn * 32 + l31;
        chan_consts(bf2f(cst[cl]), bf2f(cst[128 + cl]), bf2f(cst[256 + cl]), Lr, Li, cr, ci);
    }
    STAMP(0, SSM ? 1 : 2);

    if (SSM) {
        float L2r = CMUL_R(Lr, Li, Lr, Li), L2i = CMUL_I(Lr, Li, Lr, Li);
        float L3r = CMUL_R(L2r, L2i, Lr, Li), L3i = CMUL_I(L2r, L2i, Lr, Li);
        float L4r = CMUL_R(L2r, L2i, L2r, L2i), L4i = CMUL_I(L2r, L2i, L2r, L2i);
        f32x16 vr[2], vi[2];
#pragma unroll
        for (int mi = 0; mi < 2; ++mi)
#pragma unroll
            for (int r = 0; r < 16; ++r) {
                float ar = acc[mi][0][r], ai = acc[mi][1][r];
                vr[mi][r] = cr * ar - ci * ai;
                vi[mi][r] = cr * ai + ci * ar;
            }
#pragma unroll
        for (int mi = 0; mi < 2; ++mi)
#pragma unroll
            for (int j = 0; j < 4; ++j)
#pragma unroll
                for (int i = 1; i < 4; ++i) {
                    int b = 4 * j + i;
                    float pr = vr[mi][b - 1], pi = vi[mi][b - 1];
                    vr[mi][b] += Lr * pr - Li * pi;
                    vi[mi][b] += Lr * pi + Li * pr;
                }
        float cR = 0.f, cI = 0.f;
#pragma unroll
        for (int mi = 0; mi < 2; ++mi)
#pragma unroll
            for (int j = 0; j < 4; ++j) {
                unsigned er = __float_as_uint(vr[mi][4 * j + 3]), ei = __float_as_uint(vi[mi][4 * j + 3]);
                auto sr = __builtin_amdgcn_permlane32_swap(er, er, false, false);
                auto si = __builtin_amdgcn_permlane32_swap(ei, ei, false, false);
                float E0r = __uint_as_float(sr[0]), E0i = __uint_as_float(si[0]);
                float E1r = __uint_as_float(sr[1]), E1i = __uint_as_float(si[1]);
                float c0r = cR, c0i = cI;
                float c1r = L4r * cR - L4i * cI + E0r;
                float c1i = L4r * cI + L4i * cR + E0i;
                cR = L4r * c1r - L4i * c1i + E1r;
                cI = L4r * c1i + L4i * c1r + E1i;
                float inr = lh ? c1r : c0r, ini = lh ? c1i : c0i;
                vr[mi][4 * j + 0] += Lr * inr - Li * ini;
                vi[mi][4 * j + 0] += Lr * ini + Li * inr;
                vr[mi][4 * j + 1] += L2r * inr - L2i * ini;
                vi[mi][4 * j + 1] += L2r * ini + L2i * inr;
                vr[mi][4 * j + 2] += L3r * inr - L3i * ini;
                vi[mi][4 * j + 2] += L3r * ini + L3i * inr;
                vr[mi][4 * j + 3] += L4r * inr - L4i * ini;
                vi[mi][4 * j + 3] += L4r * ini + L4i * inr;
            }
        f32x4* local = (f32x4*)(p.ws + WS_LOCAL);
#pragma unroll
        for (int mi = 0; mi < 2; ++mi)
#pragma unroll
            for (int j = 0; j < 4; ++j) {
                int tq4 = (t0 + wt * 64 + mi * 32 + 8 * j + 4 * lh) >> 2;
                size_t o = ((size_t)tq4 * PN + ch) * 2;
                st_out(&local[o], (f32x4){vr[mi][4 * j], vi[mi][4 * j], vr[mi][4 * j + 1], vi[mi][4 * j + 1]});
                st_out(&local[o + 1], (f32x4){vr[mi][4 * j + 2], vi[mi][4 * j + 2], vr[mi][4 * j + 3], vi[mi][4 * j + 3]});
            }
        if (lh == 0) {
            f32x2* agg = (f32x2*)(p.ws + WS_AGG);
            agg[(size_t)((t0 >> 6) + wt) * PN + ch] = (f32x2){cR, cI};
        }
        STAMP(0, 3);
        STAMP_DRAIN(0, 4);
    } else {
        u32x4* hid = (u32x4*)(p.ws + WS_HIDA);
#pragma unroll
        for (int fi = 0; fi < 2; ++fi) {
            int f0 = ntile * 128 + wn * 64 + fi * 32;
            if (f0 < FF) {
                int ks = f0 >> 5;
#pragma unroll
                for (int mi = 0; mi < 2; ++mi) {
                    int t = t0 + wt * 64 + mi * 32 + l31;
                    int mt = t >> 4;
#pragma unroll
                    for (int half = 0; half < 2; ++half) {
                        u32x4 pk;
#pragma unroll
                        for (int j = 0; j < 4; ++j) {
                            float v0 = acc[fi][mi][8 * half + 2 * j];
                            float v1 = acc[fi][mi][8 * half + 2 * j + 1];
                            v0 = v0 > 0.f ? v0 : 0.f;
                            v1 = v1 > 0.f ? v1 : 0.f;
                            pk[j] = pk2(v0, v1);
                        }
                        int q = 2 * half + lh;
                        st_out(&hid[((size_t)mt * KS_MLP + ks) * 64 + (t & 15) + 16 * q], pk);
                    }
                }
            }
        }
        STAMP_DRAIN(0, 5);
    }
}

__global__ __launch_bounds__(512, 2) void k1_kernel(K1Params p) {
    __shared__ __attribute__((aligned(16))) char smem[3 * K1_STAGE_BYTES + 768];
    const int tid = threadIdx.x;
    const int lane = tid & 63;
    const int wid = tid >> 6;
    int bid = blockIdx.x;

    if (bid < K1_GEMM_BLOCKS) {
        int xcd = bid & 7, idx = bid >> 3;
        int nt24 = xcd * 3 + idx / 8, mtile = idx % 8;
        if (nt24 < 10) k1_tile<true>(p, smem, mtile, nt24);
        else k1_tile<false>(p, smem, mtile, nt24 - 10);
        return;
    }
    bid -= K1_GEMM_BLOCKS;
    if (bid < K1_G_BLOCKS) {
        float* g = (float*)(p.ws + WS_G);
        const int l16 = lane & 15;
        u32x4 xv[2][5], gv[5];
#pragma unroll
        for (int i = 0; i < 5; ++i) gv[i] = *(const u32x4*)(p.gw + (l16 + 16 * i) * 8);
#pragma unroll
        for (int u = 0; u < 2; ++u) {
            const int t = ((bid * 8 + wid) * 2 + u) * 4 + (lane >> 4);
#pragma unroll
            for (int i = 0; i < 5; ++i) xv[u][i] = *(const u32x4*)(p.x + (size_t)t * DM + (l16 + 16 * i) * 8);
        }
#pragma unroll
        for (int u = 0; u < 2; ++u) {
            const int t = ((bid * 8 + wid) * 2 + u) * 4 + (lane >> 4);
            float sum = 0.f;
#pragma unroll
            for (int i = 0; i < 5; ++i)
#pragma unroll
                for (int e = 0; e < 4; ++e) {
                    sum += bfr(bflo(xv[u][i][e]) * bflo(gv[i][e]));
                    sum += bfr(bfhi(xv[u][i][e]) * bfhi(gv[i][e]));
                }
#pragma unroll
            for (int o = 8; o > 0; o >>= 1) sum += __shfl_xor(sum, o);
            if (l16 == 0) {
                float z = bfr(sum);
                g[t] = bfr(1.0f / (1.0f + expf(-z)));
            }
        }
        return;
    }
    bid -= K1_G_BLOCKS;
    if (bid < K1_W2_BLOCKS) {
        u32x4* w2t = (u32x4*)(p.ws + WS_W2T);
        char* img = smem + wid * 8192;
        u32x4 v[2][4];
#pragma unroll
        for (int u = 0; u < 2; ++u) {
            int task = (bid * 8 + wid) * 2 + u;
            if (task > KS_MLP * 10 - 1) task = KS_MLP * 10 - 1;
            int ks = task / 10, ng = task % 10;
#pragma unroll
            for (int i = 0; i < 4; ++i) {
                int idx = lane + 64 * i, row = idx >> 3, c8 = idx & 7;
                v[u][i] = *(const u32x4*)(p.w2 + (size_t)(32 * ks + row) * DM + ng * 64 + c8 * 8);
            }
        }
#pragma unroll
        for (int u = 0; u < 2; ++u)
#pragma unroll
            for (int i = 0; i < 4; ++i) {
                int idx = lane + 64 * i, row = idx >> 3, c8 = idx & 7;
                *(u32x4*)(img + u * 4096 + row * 128 + c8 * 16) = v[u][i];
            }
        __builtin_amdgcn_fence(__ATOMIC_RELEASE, "wavefront");
        __builtin_amdgcn_wave_barrier();
        __builtin_amdgcn_fence(__ATOMIC_ACQUIRE, "wavefront");
        const int q = lane >> 4, qq = (lane & 15) >> 2, pp = lane & 3;
#pragma unroll
        for (int u = 0; u < 2; ++u) {
            int task = (bid * 8 + wid) * 2 + u;
            if (task < KS_MLP * 10) {
                int ks = task / 10, ng = task % 10;
#pragma unroll
                for (int c = 0; c < 4; ++c) {
                    int r0 = 16 * (q >> 1) + 4 * (q & 1);
                    unsigned a0 = u * 4096 + (r0 + qq) * 128 + c * 32 + pp * 8;
                    unsigned a1 = a0 + 8 * 128;
                    bf16x4 lo = __builtin_amdgcn_ds_read_tr16_b64_v4bf16((lds_bf16x4_ptr)(img + a0));
                    bf16x4 hi = __builtin_amdgcn_ds_read_tr16_b64_v4bf16((lds_bf16x4_ptr)(img + a1));
                    bf16x8 f = __builtin_shufflevector(lo, hi, 0, 1, 2, 3, 4, 5, 6, 7);
                    w2t[((size_t)(ng * 4 + c) * KS_MLP + ks) * 64 + lane] = __builtin_bit_cast(u32x4, f);
                }
            }
        }
        return;
    }
    bid -= K1_W2_BLOCKS;
    {
        u32x4* ct = (u32x4*)(p.ws + WS_CT);
        u32x4 re[4], im[4];
#pragma unroll
        for (int u = 0; u < 4; ++u) {
            int task = (bid * 8 + wid) * 4 + u;
            int nt = task / 20, pg = task % 20;
            int d = nt * 16 + (lane & 15), pc = pg * 32 + (lane >> 4) * 8;
            re[u] = *(const u32x4*)(p.cre + (size_t)d * PN + pc);
            im[u] = *(const u32x4*)(p.cim + (size_t)d * PN + pc);
        }
#pragma unroll
        for (int u = 0; u < 4; ++u) {
            int task = (bid * 8 + wid) * 4 + u;
            int nt = task / 20, pg = task % 20;
            int c = lane >> 4;
            int ks = pg * 2 + (c >> 1), q0 = 2 * (c & 1);
            u32x4 pa, pb;
            pa[0] = pk2(2.0f * bflo(re[u][0]), -2.0f * bflo(im[u][0]));
            pa[1] = pk2(2.0f * bfhi(re[u][0]), -2.0f * bfhi(im[u][0]));
            pa[2] = pk2(2.0f * bflo(re[u][1]), -2.0f * bflo(im[u][1]));
            pa[3] = pk2(2.0f * bfhi(re[u][1]), -2.0f * bfhi(im[u][1]));
            pb[0] = pk2(2.0f * bflo(re[u][2]), -2.0f * bflo(im[u][2]));
            pb[1] = pk2(2.0f * bfhi(re[u][2]), -2.0f * bfhi(im[u][2]));
            pb[2] = pk2(2.0f * bflo(re[u][3]), -2.0f * bflo(im[u][3]));
            pb[3] = pk2(2.0f * bfhi(re[u][3]), -2.0f * bfhi(im[u][3]));
            size_t o = ((size_t)nt * KS_SSM + ks) * 64 + (lane & 15);
            ct[o + 16 * q0] = pa;
            ct[o + 16 * (q0 + 1)] = pb;
        }
    }
}

struct Frag9 { u32x4 a[4]; u32x4 b[5]; };

template <int KS>
__device__ __forceinline__ void g2_load(Frag9& f, const u32x4* __restrict__ ap, const u32x4* __restrict__ bp, int ks) {
#pragma unroll
    for (int i = 0; i < 4; ++i) f.a[i] = ap[((size_t)i * KS + ks) * 64];
#pragma unroll
    for (int j = 0; j < 5; ++j) f.b[j] = bp[((size_t)j * KS + ks) * 64];
    __builtin_amdgcn_sched_barrier(0);
}
__device__ __forceinline__ void g2_mma(const Frag9& f, f32x4 (&acc)[4][5]) {
#pragma unroll
    for (int i = 0; i < 4; ++i)
#pragma unroll
        for (int j = 0; j < 5; ++j)
            acc[i][j] = __builtin_amdgcn_mfma_f32_16x16x32_bf16(__builtin_bit_cast(bf16x8, f.b[j]),
                                                                __builtin_bit_cast(bf16x8, f.a[i]), acc[i][j], 0, 0, 0);
    __builtin_amdgcn_sched_barrier(0);
}

template <int KS, int NW, int MODE>
__device__ __forceinline__ void gemm2_body(const u32x4* __restrict__ A, const u32x4* __restrict__ B,
                                           int mb, int nb, float* red, const float* __restrict__ g,
                                           const unsigned short* __restrict__ x,
                                           const unsigned short* __restrict__ dskip, float* __restrict__ out,
                                           char* stamp_base, int kid) {
    STAMP(kid, 0);
    const int lane = threadIdx.x & 63;
    const int wid = __builtin_amdgcn_readfirstlane(threadIdx.x >> 6);
    constexpr int NS = (KS + NW - 1) / NW;
    f32x4 acc[4][5];
#pragma unroll
    for (int i = 0; i < 4; ++i)
#pragma unroll
        for (int j = 0; j < 5; ++j) acc[i][j] = (f32x4){0.f, 0.f, 0.f, 0.f};
    const u32x4* ap = A + ((size_t)(mb * 4) * KS) * 64 + lane;
    const u32x4* bp = B + ((size_t)(nb * 5) * KS) * 64 + lane;
    Frag9 f0, f1, f2, f3;
    g2_load<KS>(f0, ap, bp, wid);
    g2_load<KS>(f1, ap, bp, wid + NW);
    g2_load<KS>(f2, ap, bp, wid + 2 * NW);
    g2_load<KS>(f3, ap, bp, wid + 3 * NW);
    STAMP(kid, 1);
#pragma unroll
    for (int s = 0; s < NS; ++s) {
        const int kn = wid + NW * (s + 4);
        const bool last_ragged = (s == NS - 1) && (KS % NW != 0);
        const bool do_mma = !last_ragged || (wid + NW * s < KS);
        if ((s & 3) == 0) { if (do_mma) g2_mma(f0, acc); if (s + 4 < NS) g2_load<KS>(f0, ap, bp, kn < KS ? kn : KS - 1); }
        if ((s & 3) == 1) { if (do_mma) g2_mma(f1, acc); if (s + 4 < NS) g2_load<KS>(f1, ap, bp, kn < KS ? kn : KS - 1); }
        if ((s & 3) == 2) { if (do_mma) g2_mma(f2, acc); if (s + 4 < NS) g2_load<KS>(f2, ap, bp, kn < KS ? kn : KS - 1); }
        if ((s & 3) == 3) { if (do_mma) g2_mma(f3, acc); if (s + 4 < NS) g2_load<KS>(f3, ap, bp, kn < KS ? kn : KS - 1); }
    }
    STAMP(kid, 2);
    WSTAMP(kid, 0);
    constexpr int NOWN = (20 + NW - 1) / NW;
    f32x4 eo[NOWN];
    u32x2 ex[NOWN], ed[NOWN];
    float eg[NOWN];
#pragma unroll
    for (int o = 0; o < NOWN; ++o) {
        int tau = wid + NW * o;
        if (tau > 19) tau = 19;
        int i = tau / 5, j = tau % 5;
        int t = mb * 64 + i * 16 + (lane & 15);
        int d = nb * 80 + j * 16 + (lane >> 4) * 4;
        eg[o] = g[t];
        if (MODE == 1) {
            eo[o] = *(const f32x4*)(out + (size_t)t * DM + d);
            ex[o] = *(const u32x2*)(x + (size_t)t * DM + d);
            ed[o] = *(const u32x2*)(dskip + d);
        }
    }
    f32x4* r4 = (f32x4*)red;
#pragma unroll
    for (int tau = 0; tau < 20; ++tau) {
        const int owner = tau % NW;
        if (wid != owner) {
            int wslot = wid < owner ? wid : wid - 1;
            r4[(tau * (NW - 1) + wslot) * 64 + lane] = acc[tau / 5][tau % 5];
        }
    }
    WSTAMP(kid, 1);
    __syncthreads();
    WSTAMP(kid, 2);
#pragma unroll
    for (int tau = 0; tau < 20; ++tau) {
        if (wid == tau % NW) {
            const int o = tau / NW;
            f32x4 sacc = acc[tau / 5][tau % 5];
#pragma unroll
            for (int w = 0; w < NW - 1; ++w) sacc += r4[(tau * (NW - 1) + w) * 64 + lane];
            const int i = tau / 5, j = tau % 5;
            int t = mb * 64 + i * 16 + (lane & 15);
            int d = nb * 80 + j * 16 + (lane >> 4) * 4;
            f32x4 res;
            if (MODE == 0) {
                float omg = bfr(1.0f - eg[o]);
#pragma unroll
                for (int r = 0; r < 4; ++r) res[r] = bfr(omg * bfr(sacc[r]));
            } else {
                float s0 = bfr(bflo(ex[o][0]) * bflo(ed[o][0]));
                float s1 = bfr(bfhi(ex[o][0]) * bfhi(ed[o][0]));
                float s2 = bfr(bflo(ex[o][1]) * bflo(ed[o][1]));
                float s3 = bfr(bfhi(ex[o][1]) * bfhi(ed[o][1]));
                res[0] = eg[o] * (sacc[0] + s0) + eo[o][0];
                res[1] = eg[o] * (sacc[1] + s1) + eo[o][1];
                res[2] = eg[o] * (sacc[2] + s2) + eo[o][2];
                res[3] = eg[o] * (sacc[3] + s3) + eo[o][3];
            }
            st_out((f32x4*)(out + (size_t)t * DM + d), res);
        }
    }
    WSTAMP(kid, 3);
    STAMP(kid, 3);
    STAMP_DRAIN(kid, 4);
}

#define K2_FIX_BLOCKS (NCHUNK * 5)
#define K2_GEMM_BLOCKS 256
struct K2Params {
    const unsigned short* lre;
    const unsigned short* lim;
    const unsigned short* ldt;
    char* ws;
    float* out;
};

__global__ __launch_bounds__(256, 2) void k2_kernel(K2Params p) {
    __shared__ __attribute__((aligned(16))) float smem[16384];
    const int tid = threadIdx.x, lane = tid & 63, wid = tid >> 6;
    int bid = blockIdx.x;
    if (bid >= K2_FIX_BLOCKS) {
        bid -= K2_FIX_BLOCKS;
        int xcd = bid & 7, idx = bid >> 3;
        int mb = xcd * 4 + (idx >> 3), nb = idx & 7;
        gemm2_body<KS_MLP, 4, 0>((const u32x4*)(p.ws + WS_HIDA), (const u32x4*)(p.ws + WS_W2T), mb, nb, smem,
                                 (const float*)(p.ws + WS_G), nullptr, nullptr, p.out, p.ws, 1);
        return;
    }
    {
        const int c = bid / 5, cg = bid % 5;
        f32x2* corr = (f32x2*)smem;
        const f32x4* local = (const f32x4*)(p.ws + WS_LOCAL);
        f32x4 lv[2][8];
#pragma unroll
        for (int u = 0; u < 2; ++u) {
            int uid = tid + 256 * u;
            int cq = uid & 31, tq4 = uid >> 5;
            size_t o = ((size_t)(c * 16 + tq4) * PN + cg * 128 + cq * 4) * 2;
#pragma unroll
            for (int e = 0; e < 8; ++e) lv[u][e] = local[o + e];
        }
        {
            const int chl = tid & 127, part = tid >> 7;
            const int ch = cg * 128 + chl;
            const f32x2* agg = (const f32x2*)(p.ws + WS_AGG);
            float Lr, Li;
            unsigned short a0 = p.lre[ch], a1 = p.lim[ch], a2 = p.ldt[ch];
            f32x2 e[NCHUNK - 1];
#pragma unroll
            for (int cc = 0; cc < NCHUNK - 1; ++cc) {
                int ci2 = cc < c ? cc : 0;
                e[cc] = agg[(size_t)ci2 * PN + ch];
            }
            chan_lam(bf2f(a0), bf2f(a1), bf2f(a2), Lr, Li);
            float Pr = Lr, Pi = Li;
#pragma unroll
            for (int i = 0; i < 5; ++i) {
                float nr = Pr * Pr - Pi * Pi, ni = 2.f * Pr * Pi;
                Pr = nr; Pi = ni;
            }
            const float Qr = Pr, Qi = Pi;
            Pr = Qr * Qr - Qi * Qi; Pi = 2.f * Qr * Qi;
            float Hr = 0.f, Hi = 0.f;
#pragma unroll
            for (int cc = 0; cc < NCHUNK - 1; ++cc) {
                if (cc < c) {
                    float nr = Pr * Hr - Pi * Hi + e[cc][0];
                    float ni = Pr * Hi + Pi * Hr + e[cc][1];
                    Hr = nr; Hi = ni;
                }
            }
            if (part) { float nr = Qr * Hr - Qi * Hi, ni = Qr * Hi + Qi * Hr; Hr = nr; Hi = ni; }
#pragma unroll 8
            for (int n = 0; n < 32; ++n) {
                float nr = Lr * Hr - Li * Hi, ni = Lr * Hi + Li * Hr;
                Hr = nr; Hi = ni;
                corr[(part * 32 + n) * 128 + chl] = (f32x2){Hr, Hi};
            }
        }
        __syncthreads();
        u32x4* ha = (u32x4*)(p.ws + WS_HA);
#pragma unroll
        for (int u = 0; u < 2; ++u) {
            int uid = tid + 256 * u;
            int cq = uid & 31, tq4 = uid >> 5;
            int chl = cq * 4;
#pragma unroll
            for (int tt = 0; tt < 4; ++tt) {
                int tl = tq4 * 4 + tt;
                f32x4 c0 = *(const f32x4*)&corr[tl * 128 + chl];
                f32x4 c1 = *(const f32x4*)&corr[tl * 128 + chl + 2];
                u32x4 pk;
                pk[0] = pk2(lv[u][0 + (tt >> 1)][2 * (tt & 1)] + c0[0], lv[u][0 + (tt >> 1)][2 * (tt & 1) + 1] + c0[1]);
                pk[1] = pk2(lv[u][2 + (tt >> 1)][2 * (tt & 1)] + c0[2], lv[u][2 + (tt >> 1)][2 * (tt & 1) + 1] + c0[3]);
                pk[2] = pk2(lv[u][4 + (tt >> 1)][2 * (tt & 1)] + c1[0], lv[u][4 + (tt >> 1)][2 * (tt & 1) + 1] + c1[1]);
                pk[3] = pk2(lv[u][6 + (tt >> 1)][2 * (tt & 1)] + c1[2], lv[u][6 + (tt >> 1)][2 * (tt & 1) + 1] + c1[3]);
                int t = c * 64 + tl;
                int ks = cg * 8 + (cq >> 2), q = cq & 3, mt = t >> 4;
                st_out(&ha[((size_t)mt * KS_SSM + ks) * 64 + (t & 15) + 16 * q], pk);
            }
        }
    }
}

struct K3Params {
    const unsigned short* x;
    const unsigned short* dskip;
    char* ws;
    float* out;
};

__global__ __launch_bounds__(256, 2) void k3_kernel(K3Params p) {
    __shared__ __attribute__((aligned(16))) float smem[20 * 3 * 256];
    int bid = blockIdx.x;
    int xcd = bid & 7, idx = bid >> 3;
    int mb = xcd * 4 + (idx >> 3), nb = idx & 7;
    gemm2_body<KS_SSM, 4, 1>((const u32x4*)(p.ws + WS_HA), (const u32x4*)(p.ws + WS_CT), mb, nb, smem,
                             (const float*)(p.ws + WS_G), p.x, p.dskip, p.out, p.ws, 2);
}

extern "C" void kernel_launch(void* const* d_in, const int* in_sizes, int n_in, void* d_out, int out_size,
                              void* d_ws, size_t ws_size, hipStream_t stream) {
    const unsigned short* x = (const unsigned short*)d_in[0];
    const unsigned short* lre = (const unsigned short*)d_in[1];
    const unsigned short* lim = (const unsigned short*)d_in[2];
    const unsigned short* bre = (const unsigned short*)d_in[3];
    const unsigned short* bim = (const unsigned short*)d_in[4];
    const unsigned short* cre = (const unsigned short*)d_in[5];
    const unsigned short* cim = (const unsigned short*)d_in[6];
    const unsigned short* ldt = (const unsigned short*)d_in[7];
    const unsigned short* dskip = (const unsigned short*)d_in[8];
    const unsigned short* w1 = (const unsigned short*)d_in[10];
    const unsigned short* w2 = (const unsigned short*)d_in[11];
    const unsigned short* gw = (const unsigned short*)d_in[12];
    char* ws = (char*)d_ws;
    float* out = (float*)d_out;

    K1Params p1{x, lre, lim, bre, bim, cre, cim, ldt, w1, w2, gw, ws};
    k1_kernel<<<K1_GRID, 512, 0, stream>>>(p1);
    K2Params p2{lre, lim, ldt, ws, out};
    k2_kernel<<<K2_GEMM_BLOCKS + K2_FIX_BLOCKS, 256, 0, stream>>>(p2);
    K3Params p3{x, dskip, ws, out};
    k3_kernel<<<256, 256, 0, stream>>>(p3);
}
```

```cpp
#include <hip/hip_runtime.h>
#include <hip/hip_bf16.h>
#include <stdint.h>

typedef __attribute__((ext_vector_type(8))) __bf16 bf16x8;
typedef __attribute__((ext_vector_type(4))) __bf16 bf16x4;
typedef __attribute__((ext_vector_type(16))) float f32x16;
typedef __attribute__((ext_vector_type(4))) float f32x4;
typedef __attribute__((ext_vector_type(2))) float f32x2;
typedef __attribute__((ext_vector_type(4))) unsigned u32x4;
typedef __attribute__((ext_vector_type(2))) unsigned u32x2;
typedef __attribute__((address_space(3))) bf16x4* lds_bf16x4_ptr;

#define LSEQ 2048
#define DM 640
#define PN 640
#define FF 1728
#define CHUNK 64
#define NCHUNK (LSEQ / CHUNK)
#define KS_MLP (FF / 32)
#define KS_SSM (2 * PN / 32)
#define MT (LSEQ / 16)
#define NT (DM / 16)

#define WS_GRAN 0
#define WS_GRAN_SZ ((size_t)8 * PN * 2 * 8)
#define WS_HIDA (WS_GRAN + WS_GRAN_SZ)
#define WS_HIDA_SZ ((size_t)MT * KS_MLP * 1024)
#define WS_HA (WS_HIDA + WS_HIDA_SZ)
#define WS_HA_SZ ((size_t)MT * KS_SSM * 1024)
#define WS_W2T (WS_HA + WS_HA_SZ)
#define WS_W2T_SZ ((size_t)NT * KS_MLP * 1024)
#define WS_CT (WS_W2T + WS_W2T_SZ)
#define WS_CT_SZ ((size_t)NT * KS_SSM * 1024)
#define WS_G (WS_CT + WS_CT_SZ)

#define STAMP(kid, idx) do {} while (0)
#define STAMP_DRAIN(kid, idx) do {} while (0)
#define WSTAMP(kid, idx) do {} while (0)

__device__ __forceinline__ float bf2f(unsigned short u) { return __uint_as_float(((unsigned)u) << 16); }
__device__ __forceinline__ float bflo(unsigned u) { return __uint_as_float(u << 16); }
__device__ __forceinline__ float bfhi(unsigned u) { return __uint_as_float(u & 0xffff0000u); }
__device__ __forceinline__ float bfr(float x) { return (float)(__bf16)x; }
__device__ __forceinline__ unsigned short f2bf(float x) {
    __bf16 b = (__bf16)x;
    return __builtin_bit_cast(unsigned short, b);
}
#define WT_STORES 0
template <typename T>
__device__ __forceinline__ void st_out(T* ptr, T v) {
    static_assert(sizeof(T) == 16, "16-byte stores only");
    *ptr = v;
}
__device__ __forceinline__ unsigned pk2(float a, float b) { return f2bf(a) | ((unsigned)f2bf(b) << 16); }

__device__ __forceinline__ void sincos_acc(float x, float& s, float& c) {
    float kf = rintf(x * 0.63661977236758134f);
    float r = fmaf(kf, -1.5707962513e+00f, x);
    r = fmaf(kf, -7.5497894159e-08f, r);
    r = fmaf(kf, -5.3903029534e-15f, r);
    int k = (int)kf;
    float r2 = r * r;
    float sp = fmaf(r2, -1.9515295891e-04f, 8.3321608736e-03f);
    sp = fmaf(sp, r2, -1.6666654611e-01f);
    sp = fmaf(sp * r2, r, r);
    float cp = fmaf(r2, 2.443315711809948e-05f, -1.388731625493765e-03f);
    cp = fmaf(cp, r2, 4.166664568298827e-02f);
    cp = fmaf(cp * r2, r2, fmaf(-0.5f, r2, 1.0f));
    float ss = (k & 1) ? cp : sp;
    float cc = (k & 1) ? sp : cp;
    s = (k & 2) ? -ss : ss;
    c = ((k + 1) & 2) ? -cc : cc;
}

__device__ __forceinline__ void chan_lam(float lre, float lim, float ldt, float& Lr, float& Li) {
    float dt = bfr(expf(ldt));
    float a = dt * lre, b = dt * lim;
    float ea = expf(a);
    float sb, cb;
    sincos_acc(b, sb, cb);
    Lr = ea * cb;
    Li = ea * sb;
}
__device__ __forceinline__ void chan_consts(float lre, float lim, float ldt, float& Lr, float& Li,
                                            float& cr, float& ci) {
    float dt = bfr(expf(ldt));
    float a = dt * lre, b = dt * lim;
    float ea = expf(a);
    float sb, cb;
    sincos_acc(b, sb, cb);
    Lr = ea * cb;
    Li = ea * sb;
    float sh, chh;
    sincos_acc(0.5f * b, sh, chh);
    float nr = expm1f(a) * cb - 2.0f * sh * sh;
    float ni = Li;
    float den = lre * lre + lim * lim;
    float inv = 1.0f / den;
    cr = (nr * lre + ni * lim) * inv;
    ci = (ni * lre - nr * lim) * inv;
}

#define CMUL_R(ar, ai, br, bi) ((ar) * (br) - (ai) * (bi))
#define CMUL_I(ar, ai, br, bi) ((ar) * (bi) + (ai) * (br))

#define K1_GEMM_BLOCKS 192
#define K1_G_BLOCKS 32
#define K1_W2_BLOCKS 34
#define K1_C_BLOCKS 25
#define K1_GRID (K1_GEMM_BLOCKS + K1_G_BLOCKS + K1_W2_BLOCKS + K1_C_BLOCKS)

__device__ __forceinline__ unsigned swz128(unsigned row, unsigned ch) {
    return row * 128u + ((ch ^ ((row >> 1) & 7u)) << 4);
}
__device__ __forceinline__ unsigned off_b(unsigned row, unsigned ch) {
    return 256u * row + 16u * (ch ^ (((row & 3u) << 2) | ((row >> 2) & 3u)));
}

struct K1Params {
    const unsigned short* x;
    const unsigned short* lre;
    const unsigned short* lim;
    const unsigned short* bre;
    const unsigned short* bim;
    const unsigned short* cre;
    const unsigned short* cim;
    const unsigned short* ldt;
    const unsigned short* w1;
    const unsigned short* w2;
    const unsigned short* gw;
    char* ws;
};

__device__ __forceinline__ bf16x4 tr_read_asm(unsigned lds_addr) {
    bf16x4 r;
    asm volatile("ds_read_b64_tr_b16 %0, %1" : "=v"(r) : "v"(lds_addr) : "memory");
    return r;
}
__device__ __forceinline__ bf16x8 lds_read128_asm(unsigned lds_addr) {
    bf16x8 r;
    asm volatile("ds_read_b128 %0, %1" : "=v"(r) : "v"(lds_addr) : "memory");
    return r;
}
__device__ __forceinline__ unsigned lds_addr_of(const char* ptr) {
    return (unsigned)(size_t)(__attribute__((address_space(3))) const char*)ptr;
}
#define K1_STAGE_BYTES 49152
#define K1_CONST_OFF (3 * K1_STAGE_BYTES)
#define RAW_BARRIER() do { asm volatile("s_waitcnt lgkmcnt(0)" ::: "memory"); __builtin_amdgcn_s_barrier(); } while (0)

template <bool SSM>
__device__ __forceinline__ void k1_tile(const K1Params& p, char* smem, int mtile, int ntile) {
    const int tid = threadIdx.x;
    const int lane = tid & 63;
    const int wid = __builtin_amdgcn_readfirstlane(tid >> 6);
    const int t0 = mtile * 256;
    const int wt = wid >> 1, wn = wid & 1;
    char* stamp_base = p.ws;
    STAMP(0, 0);

    const unsigned short* xs_src[4];
#pragma unroll
    for (int i = 0; i < 4; ++i) {
        unsigned row = (4 * wid + i) * 8 + (lane >> 3);
        unsigned chk = (lane & 7) ^ ((row >> 1) & 7);
        xs_src[i] = p.x + (size_t)(t0 + row) * DM + chk * 8;
    }
    const unsigned short* ws_src[2];
#pragma unroll
    for (int i = 0; i < 2; ++i) {
        if (SSM) {
            unsigned row = (2 * wid + i) * 8 + (lane >> 3);
            unsigned chk = (lane & 7) ^ ((row >> 1) & 7);
            unsigned grp = row >> 5;
            const unsigned short* base = (grp & 1) ? p.bim : p.bre;
            ws_src[i] = base + (size_t)(ntile * 64 + (row & 31) + 32 * (grp >> 1)) * DM + chk * 8;
        } else {
            unsigned row = (2 * wid + i) * 4 + (lane >> 4);
            unsigned chk = (lane & 15) ^ (((row & 3u) << 2) | ((row >> 2) & 3u));
            int n = ntile * 128 + chk * 8;
            if (n > FF - 8) n = FF - 8;
            ws_src[i] = p.w1 + (size_t)row * FF + n;
        }
    }
#define K1_GLDS(KT)                                                                                  \
    do {                                                                                             \
        char* st_ = smem + ((KT) % 3) * K1_STAGE_BYTES;                                              \
        _Pragma("unroll") for (int i = 0; i < 4; ++i)                                                \
            __builtin_amdgcn_global_load_lds((const unsigned*)(xs_src[i] + (KT) * 64),               \
                                             (unsigned*)(st_ + (4 * wid + i) * 1024), 16, 0, 0);     \
        _Pragma("unroll") for (int i = 0; i < 2; ++i)                                                \
            __builtin_amdgcn_global_load_lds((const unsigned*)(ws_src[i] + (SSM ? (size_t)(KT) * 64 : (size_t)(KT) * 64 * FF)), \
                                             (unsigned*)(st_ + 32768 + (2 * wid + i) * 1024), 16, 0, 0); \
    } while (0)

    if (SSM) {
        const int cc = ntile * 64 + 2 * (lane & 31);
        __builtin_amdgcn_global_load_lds((const unsigned*)(p.lre + cc), (unsigned*)(smem + K1_CONST_OFF), 4, 0, 0);
        __builtin_amdgcn_global_load_lds((const unsigned*)(p.lim + cc), (unsigned*)(smem + K1_CONST_OFF + 256), 4, 0, 0);
        __builtin_amdgcn_global_load_lds((const unsigned*)(p.ldt + cc), (unsigned*)(smem + K1_CONST_OFF + 512), 4, 0, 0);
    }
    K1_GLDS(0);
    K1_GLDS(1);
    K1_GLDS(2);

    f32x16 acc[2][2];
#pragma unroll
    for (int i = 0; i < 2; ++i)
#pragma unroll
        for (int j = 0; j < 2; ++j)
#pragma unroll
            for (int r = 0; r < 16; ++r) acc[i][j][r] = 0.f;

    const unsigned l31 = lane & 31, lh = lane >> 5;
    const int ch = ntile * 64 + wn * 32 + l31;
    const unsigned xa0 = wt * 64 + l31, xa1 = xa0 + 32;
    const unsigned ba0 = wn * 64 + l31, ba1 = ba0 + 32;
    const unsigned tq = (lane & 15) >> 2, tp = lane & 3, tblk = (lane >> 4) & 1;

#define K1_FRAGS(F, T, XS, WSM, S)                                                                   \
    do {                                                                                             \
        const unsigned xb_ = lds_addr_of(XS);                                                        \
        const unsigned wb_ = lds_addr_of(WSM);                                                       \
        F[0] = lds_read128_asm(xb_ + swz128(xa0, 2 * (S) + lh));                                     \
        F[1] = lds_read128_asm(xb_ + swz128(xa1, 2 * (S) + lh));                                     \
        if (SSM) {                                                                                   \
            F[2] = lds_read128_asm(wb_ + swz128(ba0, 2 * (S) + lh));                                 \
            F[3] = lds_read128_asm(wb_ + swz128(ba1, 2 * (S) + lh));                                 \
        } else {                                                                                     \
            _Pragma("unroll") for (int i = 0; i < 2; ++i) {                                          \
                unsigned c = 2 * wn + i;                                                             \
                unsigned a0 = off_b(16 * (S) + 8 * lh + 0 + tq, 4 * c + 2 * tblk + (tp >> 1)) + 8 * (tp & 1); \
                unsigned a1 = off_b(16 * (S) + 8 * lh + 4 + tq, 4 * c + 2 * tblk + (tp >> 1)) + 8 * (tp & 1); \
                T[2 * i] = tr_read_asm(wb_ + a0);                                                    \
                T[2 * i + 1] = tr_read_asm(wb_ + a1);                                                \
            }                                                                                        \
        }                                                                                            \
    } while (0)
#define K1_TWAIT(F, T, LAST)                                                                         \
    do {                                                                                             \
        if (SSM) {                                                                                   \
            if (LAST) asm volatile("s_waitcnt lgkmcnt(0)" : "+v"(F[0]), "+v"(F[1]), "+v"(F[2]), "+v"(F[3]) :: "memory"); \
            else asm volatile("s_waitcnt lgkmcnt(4)" : "+v"(F[0]), "+v"(F[1]), "+v"(F[2]), "+v"(F[3]) :: "memory"); \
        } else {                                                                                     \
            if (LAST) asm volatile("s_waitcnt lgkmcnt(0)" : "+v"(F[0]), "+v"(F[1]), "+v"(T[0]), "+v"(T[1]), "+v"(T[2]), "+v"(T[3]) :: "memory"); \
            else asm volatile("s_waitcnt lgkmcnt(6)" : "+v"(F[0]), "+v"(F[1]), "+v"(T[0]), "+v"(T[1]), "+v"(T[2]), "+v"(T[3]) :: "memory"); \
            F[2] = __builtin_shufflevector(T[0], T[1], 0, 1, 2, 3, 4, 5, 6, 7);                      \
            F[3] = __builtin_shufflevector(T[2], T[3], 0, 1, 2, 3, 4, 5, 6, 7);                      \
        }                                                                                            \
    } while (0)
#define K1_MMA(F)                                                                                    \
    do {                                                                                             \
        if (SSM) {                                                                                   \
            acc[0][0] = __builtin_amdgcn_mfma_f32_32x32x16_bf16(F[0], F[2], acc[0][0], 0, 0, 0);     \
            acc[0][1] = __builtin_amdgcn_mfma_f32_32x32x16_bf16(F[0], F[3], acc[0][1], 0, 0, 0);     \
            acc[1][0] = __builtin_amdgcn_mfma_f32_32x32x16_bf16(F[1], F[2], acc[1][0], 0, 0, 0);     \
            acc[1][1] = __builtin_amdgcn_mfma_f32_32x32x16_bf16(F[1], F[3], acc[1][1], 0, 0, 0);     \
        } else {                                                                                     \
            acc[0][0] = __builtin_amdgcn_mfma_f32_32x32x16_bf16(F[2], F[0], acc[0][0], 0, 0, 0);     \
            acc[0][1] = __builtin_amdgcn_mfma_f32_32x32x16_bf16(F[2], F[1], acc[0][1], 0, 0, 0);     \
            acc[1][0] = __builtin_amdgcn_mfma_f32_32x32x16_bf16(F[3], F[0], acc[1][0], 0, 0, 0);     \
            acc[1][1] = __builtin_amdgcn_mfma_f32_32x32x16_bf16(F[3], F[1], acc[1][1], 0, 0, 0);     \
        }                                                                                            \
    } while (0)
    bf16x8 fA[4], fB[4];
    bf16x4 tA[4], tB[4];
#define K1_STEP(KT)                                                                                  \
    do {                                                                                             \
        char* xs = smem + ((KT) % 3) * K1_STAGE_BYTES;                                               \
        char* wsm = xs + 32768;                                                                      \
        char* xn = smem + (((KT) + 1) % 3) * K1_STAGE_BYTES;                                         \
        char* wn_ = xn + 32768;                                                                      \
        K1_TWAIT(fA, tA, 0);                                                                         \
        K1_MMA(fA);                                                                                  \
        __builtin_amdgcn_sched_barrier(0);                                                           \
        K1_FRAGS(fA, tA, xs, wsm, 2);                                                                \
        __builtin_amdgcn_sched_barrier(0);                                                           \
        K1_TWAIT(fB, tB, 0);                                                                         \
        K1_MMA(fB);                                                                                  \
        __builtin_amdgcn_sched_barrier(0);                                                           \
        K1_FRAGS(fB, tB, xs, wsm, 3);                                                                \
        __builtin_amdgcn_sched_barrier(0);                                                           \
        K1_TWAIT(fA, tA, 0);                                                                         \
        K1_MMA(fA);                                                                                  \
        __builtin_amdgcn_sched_barrier(0);                                                           \
        if ((KT) + 1 < 10) {                                                                         \
            if ((KT) + 2 < 10) asm volatile("s_waitcnt vmcnt(6)" ::: "memory");                      \
            else asm volatile("s_waitcnt vmcnt(0)" ::: "memory");                                    \
            RAW_BARRIER();                                                                           \
            if ((KT) + 3 < 10) K1_GLDS((KT) + 3);                                                    \
            K1_FRAGS(fA, tA, xn, wn_, 0);                                                            \
            __builtin_amdgcn_sched_barrier(0);                                                       \
            K1_TWAIT(fB, tB, 0);                                                                     \
        } else {                                                                                     \
            K1_TWAIT(fB, tB, 1);                                                                     \
        }                                                                                            \
        K1_MMA(fB);                                                                                  \
        __builtin_amdgcn_sched_barrier(0);                                                           \
        if ((KT) + 1 < 10) K1_FRAGS(fB, tB, xn, wn_, 1);                                             \
        __builtin_amdgcn_sched_barrier(0);                                                           \
        STAMP(0, 8 + (KT));                                                                          \
    } while (0)
    asm volatile("s_waitcnt vmcnt(12)" ::: "memory");
    RAW_BARRIER();
    K1_FRAGS(fA, tA, smem, smem + 32768, 0);
    K1_FRAGS(fB, tB, smem, smem + 32768, 1);
    __builtin_amdgcn_sched_barrier(0);
    K1_STEP(0);
    K1_STEP(1);
    K1_STEP(2);
    K1_STEP(3);
    K1_STEP(4);
    K1_STEP(5);
    K1_STEP(6);
    K1_STEP(7);
    K1_STEP(8);
    K1_STEP(9);
#undef K1_STEP
#undef K1_FRAGS
#undef K1_TWAIT
#undef K1_MMA
#undef K1_GLDS
    float Lr = 0.f, Li = 0.f, cr = 0.f, ci = 0.f;
    if (SSM) {
        const unsigned short* cst = (const unsigned short*)(smem + K1_CONST_OFF);
        const int cl = wn * 32 + l31;
        chan_consts(bf2f(cst[cl]), bf2f(cst[128 + cl]), bf2f(cst[256 + cl]), Lr, Li, cr, ci);
    }
    STAMP(0, SSM ? 1 : 2);

    if (SSM) {
        float L2r = CMUL_R(Lr, Li, Lr, Li), L2i = CMUL_I(Lr, Li, Lr, Li);
        float L3r = CMUL_R(L2r, L2i, Lr, Li), L3i = CMUL_I(L2r, L2i, Lr, Li);
        float L4r = CMUL_R(L2r, L2i, L2r, L2i), L4i = CMUL_I(L2r, L2i, L2r, L2i);
        f32x16 vr[2], vi[2];
#pragma unroll
        for (int mi = 0; mi < 2; ++mi)
#pragma unroll
            for (int r = 0; r < 16; ++r) {
                float ar = acc[mi][0][r], ai = acc[mi][1][r];
                vr[mi][r] = cr * ar - ci * ai;
                vi[mi][r] = cr * ai + ci * ar;
            }
#pragma unroll
        for (int mi = 0; mi < 2; ++mi)
#pragma unroll
            for (int j = 0; j < 4; ++j)
#pragma unroll
                for (int i = 1; i < 4; ++i) {
                    int b = 4 * j + i;
                    float pr = vr[mi][b - 1], pi = vi[mi][b - 1];
                    vr[mi][b] += Lr * pr - Li * pi;
                    vi[mi][b] += Lr * pi + Li * pr;
                }
        float E0r[8], E0i[8], E1r[8], E1i[8];
#pragma unroll
        for (int g = 0; g < 8; ++g) {
            unsigned er = __float_as_uint(vr[g >> 2][4 * (g & 3) + 3]), ei = __float_as_uint(vi[g >> 2][4 * (g & 3) + 3]);
            auto sr = __builtin_amdgcn_permlane32_swap(er, er, false, false);
            auto si = __builtin_amdgcn_permlane32_swap(ei, ei, false, false);
            E0r[g] = __uint_as_float(sr[0]); E0i[g] = __uint_as_float(si[0]);
            E1r[g] = __uint_as_float(sr[1]); E1i[g] = __uint_as_float(si[1]);
        }
        float aR = 0.f, aI = 0.f;
#pragma unroll
        for (int g = 0; g < 8; ++g) {
            float c1r = L4r * aR - L4i * aI + E0r[g], c1i = L4r * aI + L4i * aR + E0i[g];
            aR = L4r * c1r - L4i * c1i + E1r[g];
            aI = L4r * c1i + L4i * c1r + E1i[g];
        }
        float Pr = L4r, Pi = L4i;
#pragma unroll
        for (int i = 0; i < 4; ++i) { float nr = Pr * Pr - Pi * Pi, ni = 2.f * Pr * Pi; Pr = nr; Pi = ni; }

        f32x2* aggL = (f32x2*)smem;
        f32x2* hinL = (f32x2*)(smem + 2048);
        const int cl = wn * 32 + l31;
        __syncthreads();
        if (lh == 0) aggL[wt * 64 + cl] = (f32x2){aR, aI};
        __syncthreads();
        float bR = 0.f, bI = 0.f, inR = 0.f, inI = 0.f;
#pragma unroll
        for (int w = 0; w < 4; ++w) {
            if (w == wt) { inR = bR; inI = bI; }
            f32x2 a = aggL[w * 64 + cl];
            float nr = Pr * bR - Pi * bI + a[0], ni = Pr * bI + Pi * bR + a[1];
            bR = nr; bI = ni;
        }
        if (wt == 0) {
            unsigned long long* gran = (unsigned long long*)(p.ws + WS_GRAN);
            const float myv = lh ? bI : bR;
            __hip_atomic_store(&gran[((size_t)mtile * PN + ch) * 2 + lh],
                               (1ull << 32) | (unsigned long long)__float_as_uint(myv), __ATOMIC_RELAXED,
                               __HIP_MEMORY_SCOPE_AGENT);
            float Qr = Pr * Pr - Pi * Pi, Qi = 2.f * Pr * Pi;
            { float nr = Qr * Qr - Qi * Qi, ni = 2.f * Qr * Qi; Qr = nr; Qi = ni; }
            unsigned val[7];
            for (unsigned spin = 0; spin < (1u << 24); ++spin) {
                bool ok = true;
#pragma unroll
                for (int m = 0; m < 7; ++m) {
                    if (m < mtile) {
                        unsigned long long x = __hip_atomic_load(&gran[((size_t)m * PN + ch) * 2 + lh], __ATOMIC_RELAXED,
                                                                 __HIP_MEMORY_SCOPE_AGENT);
                        val[m] = (unsigned)x;
                        ok = ok && ((x >> 32) == 1ull);
                    }
                }
                if (__all(ok)) break;
                __builtin_amdgcn_s_sleep(1);
            }
            float hR = 0.f, hI = 0.f;
#pragma unroll
            for (int m = 0; m < 7; ++m) {
                if (m < mtile) {
                    auto sw = __builtin_amdgcn_permlane32_swap(val[m], val[m], false, false);
                    float er = __uint_as_float(sw[0]), ei = __uint_as_float(sw[1]);
                    float nr = Qr * hR - Qi * hI + er, ni = Qr * hI + Qi * hR + ei;
                    hR = nr; hI = ni;
                }
            }
            if (lh == 0) hinL[cl] = (f32x2){hR, hI};
        }
        __syncthreads();
        float cR, cI;
        {
            f32x2 hb = hinL[cl];
            float hR = hb[0], hI = hb[1];
#pragma unroll
            for (int w = 0; w < 3; ++w)
                if (w < wt) { float nr = Pr * hR - Pi * hI, ni = Pr * hI + Pi * hR; hR = nr; hI = ni; }
            cR = hR + inR; cI = hI + inI;
        }
#pragma unroll
        for (int g = 0; g < 8; ++g) {
            const int mi = g >> 2, j = g & 3;
            float c0r = cR, c0i = cI;
            float c1r = L4r * cR - L4i * cI + E0r[g];
            float c1i = L4r * cI + L4i * cR + E0i[g];
            cR = L4r * c1r - L4i * c1i + E1r[g];
            cI = L4r * c1i + L4i * c1r + E1i[g];
            float inr = lh ? c1r : c0r, ini = lh ? c1i : c0i;
            vr[mi][4 * j + 0] += Lr * inr - Li * ini;
            vi[mi][4 * j + 0] += Lr * ini + Li * inr;
            vr[mi][4 * j + 1] += L2r * inr - L2i * ini;
            vi[mi][4 * j + 1] += L2r * ini + L2i * inr;
            vr[mi][4 * j + 2] += L3r * inr - L3i * ini;
            vi[mi][4 * j + 2] += L3r * ini + L3i * inr;
            vr[mi][4 * j + 3] += L4r * inr - L4i * ini;
            vi[mi][4 * j + 3] += L4r * ini + L4i * inr;
        }
        STAMP(0, 3);
        char* tile = smem + 4096 + wid * 9216;
#pragma unroll
        for (int mi = 0; mi < 2; ++mi)
#pragma unroll
            for (int r = 0; r < 16; ++r) {
                int tl = mi * 32 + (r & 3) + 8 * (r >> 2) + 4 * lh;
                *(unsigned*)(tile + tl * 144 + l31 * 4) = pk2(vr[mi][r], vi[mi][r]);
            }
        __builtin_amdgcn_fence(__ATOMIC_RELEASE, "wavefront");
        __builtin_amdgcn_wave_barrier();
        __builtin_amdgcn_fence(__ATOMIC_ACQUIRE, "wavefront");
        u32x4* ha = (u32x4*)(p.ws + WS_HA);
#pragma unroll
        for (int mq = 0; mq < 4; ++mq)
#pragma unroll
            for (int k2 = 0; k2 < 2; ++k2) {
                u32x4 v = *(const u32x4*)(tile + (mq * 16 + (lane & 15)) * 144 + (k2 * 4 + (lane >> 4)) * 16);
                int mt = ((t0 + wt * 64) >> 4) + mq;
                int ks = ((ntile * 64 + wn * 32) >> 4) + k2;
                ha[((size_t)mt * KS_SSM + ks) * 64 + lane] = v;
            }
        STAMP_DRAIN(0, 4);
    } else {
        u32x4* hid = (u32x4*)(p.ws + WS_HIDA);
#pragma unroll
        for (int fi = 0; fi < 2; ++fi) {
            int f0 = ntile * 128 + wn * 64 + fi * 32;
            if (f0 < FF) {
                int ks = f0 >> 5;
#pragma unroll
                for (int mi = 0; mi < 2; ++mi) {
                    int t = t0 + wt * 64 + mi * 32 + l31;
                    int mt = t >> 4;
#pragma unroll
                    for (int half = 0; half < 2; ++half) {
                        u32x4 pk;
#pragma unroll
                        for (int j = 0; j < 4; ++j) {
                            float v0 = acc[fi][mi][8 * half + 2 * j];
                            float v1 = acc[fi][mi][8 * half + 2 * j + 1];
                            v0 = v0 > 0.f ? v0 : 0.f;
                            v1 = v1 > 0.f ? v1 : 0.f;
                            pk[j] = pk2(v0, v1);
                        }
                        int q = 2 * half + lh;
                        st_out(&hid[((size_t)mt * KS_MLP + ks) * 64 + (t & 15) + 16 * q], pk);
                    }
                }
            }
        }
        STAMP_DRAIN(0, 5);
    }
}

__global__ __launch_bounds__(512, 2) void k1_kernel(K1Params p) {
    __shared__ __attribute__((aligned(16))) char smem[3 * K1_STAGE_BYTES + 768];
    const int tid = threadIdx.x;
    const int lane = tid & 63;
    const int wid = tid >> 6;
    int bid = blockIdx.x;

    if (bid < K1_GEMM_BLOCKS) {
        int xcd = bid & 7, idx = bid >> 3;
        int nt24 = xcd * 3 + idx / 8, mtile = idx % 8;
        if (nt24 < 10) k1_tile<true>(p, smem, mtile, nt24);
        else k1_tile<false>(p, smem, mtile, nt24 - 10);
        return;
    }
    bid -= K1_GEMM_BLOCKS;
    char* stamp_base = p.ws;
    STAMP(0, 20);
    if (bid < K1_G_BLOCKS) {
        float* g = (float*)(p.ws + WS_G);
        const int l16 = lane & 15;
        u32x4 xv[2][5], gv[5];
#pragma unroll
        for (int i = 0; i < 5; ++i) gv[i] = *(const u32x4*)(p.gw + (l16 + 16 * i) * 8);
#pragma unroll
        for (int u = 0; u < 2; ++u) {
            const int t = ((bid * 8 + wid) * 2 + u) * 4 + (lane >> 4);
#pragma unroll
            for (int i = 0; i < 5; ++i) xv[u][i] = *(const u32x4*)(p.x + (size_t)t * DM + (l16 + 16 * i) * 8);
        }
#pragma unroll
        for (int u = 0; u < 2; ++u) {
            const int t = ((bid * 8 + wid) * 2 + u) * 4 + (lane >> 4);
            float sum = 0.f;
#pragma unroll
            for (int i = 0; i < 5; ++i)
#pragma unroll
                for (int e = 0; e < 4; ++e) {
                    sum += bfr(bflo(xv[u][i][e]) * bflo(gv[i][e]));
                    sum += bfr(bfhi(xv[u][i][e]) * bfhi(gv[i][e]));
                }
#pragma unroll
            for (int o = 8; o > 0; o >>= 1) sum += __shfl_xor(sum, o);
            if (l16 == 0) {
                float z = bfr(sum);
                g[t] = bfr(1.0f / (1.0f + expf(-z)));
            }
        }
        STAMP_DRAIN(0, 21);
        return;
    }
    bid -= K1_G_BLOCKS;
    if (bid < K1_W2_BLOCKS) {
        u32x4* w2t = (u32x4*)(p.ws + WS_W2T);
        char* img = smem + wid * 8192;
        u32x4 v[2][4];
#pragma unroll
        for (int u = 0; u < 2; ++u) {
            int task = (bid * 8 + wid) * 2 + u;
            if (task > KS_MLP * 10 - 1) task = KS_MLP * 10 - 1;
            int ks = task / 10, ng = task % 10;
#pragma unroll
            for (int i = 0; i < 4; ++i) {
                int idx = lane + 64 * i, row = idx >> 3, c8 = idx & 7;
                v[u][i] = *(const u32x4*)(p.w2 + (size_t)(32 * ks + row) * DM + ng * 64 + c8 * 8);
            }
        }
#pragma unroll
        for (int u = 0; u < 2; ++u)
#pragma unroll
            for (int i = 0; i < 4; ++i) {
                int idx = lane + 64 * i, row = idx >> 3, c8 = idx & 7;
                *(u32x4*)(img + u * 4096 + row * 128 + c8 * 16) = v[u][i];
            }
        __builtin_amdgcn_fence(__ATOMIC_RELEASE, "wavefront");
        __builtin_amdgcn_wave_barrier();
        __builtin_amdgcn_fence(__ATOMIC_ACQUIRE, "wavefront");
        const int q = lane >> 4, qq = (lane & 15) >> 2, pp = lane & 3;
#pragma unroll
        for (int u = 0; u < 2; ++u) {
            int task = (bid * 8 + wid) * 2 + u;
            if (task < KS_MLP * 10) {
                int ks = task / 10, ng = task % 10;
#pragma unroll
                for (int c = 0; c < 4; ++c) {
                    int r0 = 16 * (q >> 1) + 4 * (q & 1);
                    unsigned a0 = u * 4096 + (r0 + qq) * 128 + c * 32 + pp * 8;
                    unsigned a1 = a0 + 8 * 128;
                    bf16x4 lo = __builtin_amdgcn_ds_read_tr16_b64_v4bf16((lds_bf16x4_ptr)(img + a0));
                    bf16x4 hi = __builtin_amdgcn_ds_read_tr16_b64_v4bf16((lds_bf16x4_ptr)(img + a1));
                    bf16x8 f = __builtin_shufflevector(lo, hi, 0, 1, 2, 3, 4, 5, 6, 7);
                    w2t[((size_t)(ng * 4 + c) * KS_MLP + ks) * 64 + lane] = __builtin_bit_cast(u32x4, f);
                }
            }
        }
        STAMP_DRAIN(0, 22);
        return;
    }
    bid -= K1_W2_BLOCKS;
    {
        u32x4* ct = (u32x4*)(p.ws + WS_CT);
        u32x4 re[4], im[4];
#pragma unroll
        for (int u = 0; u < 4; ++u) {
            int task = (bid * 8 + wid) * 4 + u;
            int nt = task / 20, pg = task % 20;
            int d = nt * 16 + (lane & 15), pc = pg * 32 + (lane >> 4) * 8;
            re[u] = *(const u32x4*)(p.cre + (size_t)d * PN + pc);
            im[u] = *(const u32x4*)(p.cim + (size_t)d * PN + pc);
        }
#pragma unroll
        for (int u = 0; u < 4; ++u) {
            int task = (bid * 8 + wid) * 4 + u;
            int nt = task / 20, pg = task % 20;
            int c = lane >> 4;
            int ks = pg * 2 + (c >> 1), q0 = 2 * (c & 1);
            u32x4 pa, pb;
            pa[0] = pk2(2.0f * bflo(re[u][0]), -2.0f * bflo(im[u][0]));
            pa[1] = pk2(2.0f * bfhi(re[u][0]), -2.0f * bfhi(im[u][0]));
            pa[2] = pk2(2.0f * bflo(re[u][1]), -2.0f * bflo(im[u][1]));
            pa[3] = pk2(2.0f * bfhi(re[u][1]), -2.0f * bfhi(im[u][1]));
            pb[0] = pk2(2.0f * bflo(re[u][2]), -2.0f * bflo(im[u][2]));
            pb[1] = pk2(2.0f * bfhi(re[u][2]), -2.0f * bfhi(im[u][2]));
            pb[2] = pk2(2.0f * bflo(re[u][3]), -2.0f * bflo(im[u][3]));
            pb[3] = pk2(2.0f * bfhi(re[u][3]), -2.0f * bfhi(im[u][3]));
            size_t o = ((size_t)nt * KS_SSM + ks) * 64 + (lane & 15);
            ct[o + 16 * q0] = pa;
            ct[o + 16 * (q0 + 1)] = pb;
        }
        STAMP_DRAIN(0, 23);
    }
}

struct Frag9 { u32x4 a[4]; u32x4 b[5]; };

__device__ __forceinline__ void g2_load(Frag9& f, const char* __restrict__ ap, const char* __restrict__ bp,
                                        unsigned voff, int KS, int ks) {
#pragma unroll
    for (int i = 0; i < 4; ++i) f.a[i] = *(const u32x4*)(ap + (size_t)((i * KS + ks) * 1024) + voff);
#pragma unroll
    for (int j = 0; j < 5; ++j) f.b[j] = *(const u32x4*)(bp + (size_t)((j * KS + ks) * 1024) + voff);
    __builtin_amdgcn_sched_barrier(0);
}
__device__ __forceinline__ void g2_mma(const Frag9& f, f32x4 (&acc)[4][5]) {
#pragma unroll
    for (int i = 0; i < 4; ++i)
#pragma unroll
        for (int j = 0; j < 5; ++j)
            acc[i][j] = __builtin_amdgcn_mfma_f32_16x16x32_bf16(__builtin_bit_cast(bf16x8, f.b[j]),
                                                                __builtin_bit_cast(bf16x8, f.a[i]), acc[i][j], 0, 0, 0);
    __builtin_amdgcn_sched_barrier(0);
}

template <int KS, int KSTRIDE, int NSTEPS>
__device__ __forceinline__ void g2_run(const char* __restrict__ ap, const char* __restrict__ bp, unsigned voff,
                                       int k0, f32x4 (&acc)[4][5]) {
    Frag9 f0, f1, f2, f3;
    g2_load(f0, ap, bp, voff, KS, k0);
    g2_load(f1, ap, bp, voff, KS, k0 + KSTRIDE);
    g2_load(f2, ap, bp, voff, KS, k0 + 2 * KSTRIDE);
    g2_load(f3, ap, bp, voff, KS, k0 + 3 * KSTRIDE);
#pragma unroll
    for (int st = 0; st < NSTEPS; ++st) {
        const int kn = k0 + (st + 4) * KSTRIDE;
        if ((st & 3) == 0) { g2_mma(f0, acc); if (st + 4 < NSTEPS) g2_load(f0, ap, bp, voff, KS, kn); }
        if ((st & 3) == 1) { g2_mma(f1, acc); if (st + 4 < NSTEPS) g2_load(f1, ap, bp, voff, KS, kn); }
        if ((st & 3) == 2) { g2_mma(f2, acc); if (st + 4 < NSTEPS) g2_load(f2, ap, bp, voff, KS, kn); }
        if ((st & 3) == 3) { g2_mma(f3, acc); if (st + 4 < NSTEPS) g2_load(f3, ap, bp, voff, KS, kn); }
    }
}

struct K2Params {
    const unsigned short* x;
    const unsigned short* dskip;
    char* ws;
    float* out;
};

__global__ __launch_bounds__(512, 2) void k2_kernel(K2Params p) {
    __shared__ __attribute__((aligned(16))) float red[20 * 7 * 256];
    char* stamp_base = p.ws;
    STAMP(1, 0);
    const int bid = blockIdx.x;
    const int xcd = bid & 7, idx = bid >> 3;
    const int mb = xcd * 4 + (idx >> 3), nb = idx & 7;
    const int lane = threadIdx.x & 63;
    const int wid = __builtin_amdgcn_readfirstlane(threadIdx.x >> 6);
    const unsigned voff = lane * 16;
    f32x4 acc[4][5];
#pragma unroll
    for (int i = 0; i < 4; ++i)
#pragma unroll
        for (int j = 0; j < 5; ++j) acc[i][j] = (f32x4){0.f, 0.f, 0.f, 0.f};
    if (wid < 5) {
        const char* ap = p.ws + WS_HIDA + (size_t)(mb * 4) * KS_MLP * 1024;
        const char* bp = p.ws + WS_W2T + (size_t)(nb * 5) * KS_MLP * 1024;
        if (wid < 4) g2_run<KS_MLP, 5, 11>(ap, bp, voff, wid, acc);
        else g2_run<KS_MLP, 5, 10>(ap, bp, voff, wid, acc);
    } else {
        const char* ap = p.ws + WS_HA + (size_t)(mb * 4) * KS_SSM * 1024;
        const char* bp = p.ws + WS_CT + (size_t)(nb * 5) * KS_SSM * 1024;
        if (wid == 5) g2_run<KS_SSM, 3, 14>(ap, bp, voff, 0, acc);
        else g2_run<KS_SSM, 3, 13>(ap, bp, voff, wid - 5, acc);
    }
    STAMP(1, 2);
    const float* g = (const float*)(p.ws + WS_G);
    u32x2 ex[3], ed[3];
    float eg[3];
#pragma unroll
    for (int o = 0; o < 3; ++o) {
        int tau = wid + 8 * o;
        if (tau > 19) tau = 19;
        int i = tau / 5, j = tau % 5;
        int t = mb * 64 + i * 16 + (lane & 15);
        int d = nb * 80 + j * 16 + (lane >> 4) * 4;
        eg[o] = g[t];
        ex[o] = *(const u32x2*)(p.x + (size_t)t * DM + d);
        ed[o] = *(const u32x2*)(p.dskip + d);
    }
    f32x4* r4 = (f32x4*)red;
#pragma unroll
    for (int tau = 0; tau < 20; ++tau) {
        const int owner = tau % 8;
        if (wid != owner) {
            int wslot = wid < owner ? wid : wid - 1;
            r4[(tau * 7 + wslot) * 64 + lane] = acc[tau / 5][tau % 5];
        }
    }
    __syncthreads();
#pragma unroll
    for (int tau = 0; tau < 20; ++tau) {
        if (wid == tau % 8) {
            const int o = tau / 8, owner = tau % 8;
            f32x4 sm = (f32x4){0.f, 0.f, 0.f, 0.f}, ss = (f32x4){0.f, 0.f, 0.f, 0.f};
            if (owner < 5) sm = acc[tau / 5][tau % 5]; else ss = acc[tau / 5][tau % 5];
#pragma unroll
            for (int w = 0; w < 7; ++w) {
                const int writer = w < owner ? w : w + 1;
                f32x4 v = r4[(tau * 7 + w) * 64 + lane];
                if (writer < 5) sm += v; else ss += v;
            }
            const int i = tau / 5, j = tau % 5;
            int t = mb * 64 + i * 16 + (lane & 15);
            int d = nb * 80 + j * 16 + (lane >> 4) * 4;
            const float gg = eg[o];
            const float omg = bfr(1.0f - gg);
            float s0 = bfr(bflo(ex[o][0]) * bflo(ed[o][0]));
            float s1 = bfr(bfhi(ex[o][0]) * bfhi(ed[o][0]));
            float s2 = bfr(bflo(ex[o][1]) * bflo(ed[o][1]));
            float s3 = bfr(bfhi(ex[o][1]) * bfhi(ed[o][1]));
            f32x4 res;
            res[0] = gg * (ss[0] + s0) + bfr(omg * bfr(sm[0]));
            res[1] = gg * (ss[1] + s1) + bfr(omg * bfr(sm[1]));
            res[2] = gg * (ss[2] + s2) + bfr(omg * bfr(sm[2]));
            res[3] = gg * (ss[3] + s3) + bfr(omg * bfr(sm[3]));
            *(f32x4*)(p.out + (size_t)t * DM + d) = res;
        }
    }
    STAMP(1, 3);
    STAMP_DRAIN(1, 4);
}

extern "C" void kernel_launch(void* const* d_in, const int* in_sizes, int n_in, void* d_out, int out_size,
                              void* d_ws, size_t ws_size, hipStream_t stream) {
    const unsigned short* x = (const unsigned short*)d_in[0];
    const unsigned short* lre = (const unsigned short*)d_in[1];
    const unsigned short* lim = (const unsigned short*)d_in[2];
    const unsigned short* bre = (const unsigned short*)d_in[3];
    const unsigned short* bim = (const unsigned short*)d_in[4];
    const unsigned short* cre = (const unsigned short*)d_in[5];
    const unsigned short* cim = (const unsigned short*)d_in[6];
    const unsigned short* ldt = (const unsigned short*)d_in[7];
    const unsigned short* dskip = (const unsigned short*)d_in[8];
    const unsigned short* w1 = (const unsigned short*)d_in[10];
    const unsigned short* w2 = (const unsigned short*)d_in[11];
    const unsigned short* gw = (const unsigned short*)d_in[12];
    char* ws = (char*)d_ws;
    float* out = (float*)d_out;

    (void)hipMemsetAsync(ws + WS_GRAN, 0, WS_GRAN_SZ, stream);
    K1Params p1{x, lre, lim, bre, bim, cre, cim, ldt, w1, w2, gw, ws};
    k1_kernel<<<K1_GRID, 512, 0, stream>>>(p1);
    K2Params p2{x, dskip, ws, out};
    k2_kernel<<<256, 512, 0, stream>>>(p2);
}
```

```cpp
#include <hip/hip_runtime.h>
#include <hip/hip_bf16.h>
#include <stdint.h>

typedef __attribute__((ext_vector_type(8))) __bf16 bf16x8;
typedef __attribute__((ext_vector_type(4))) __bf16 bf16x4;
typedef __attribute__((ext_vector_type(16))) float f32x16;
typedef __attribute__((ext_vector_type(4))) float f32x4;
typedef __attribute__((ext_vector_type(2))) float f32x2;
typedef __attribute__((ext_vector_type(4))) unsigned u32x4;
typedef __attribute__((ext_vector_type(2))) unsigned u32x2;
typedef __attribute__((address_space(3))) bf16x4* lds_bf16x4_ptr;

#define LSEQ 2048
#define DM 640
#define PN 640
#define FF 1728
#define CHUNK 64
#define NCHUNK (LSEQ / CHUNK)
#define KS_MLP (FF / 32)
#define KS_SSM (2 * PN / 32)
#define MT (LSEQ / 16)
#define NT (DM / 16)

#define WS_GRAN 0
#define WS_GRAN_SZ ((size_t)8 * PN * 2 * 8)
#define WS_HIDA (WS_GRAN + WS_GRAN_SZ)
#define WS_HIDA_SZ ((size_t)MT * KS_MLP * 1024)
#define WS_HA (WS_HIDA + WS_HIDA_SZ)
#define WS_HA_SZ ((size_t)MT * KS_SSM * 1024)
#define WS_W2T (WS_HA + WS_HA_SZ)
#define WS_W2T_SZ ((size_t)NT * KS_MLP * 1024)
#define WS_CT (WS_W2T + WS_W2T_SZ)
#define WS_CT_SZ ((size_t)NT * KS_SSM * 1024)
#define WS_G (WS_CT + WS_CT_SZ)

#define STAMP(kid, idx) do {} while (0)
#define STAMP_DRAIN(kid, idx) do {} while (0)
#define WSTAMP(kid, idx) do {} while (0)

__device__ __forceinline__ float bf2f(unsigned short u) { return __uint_as_float(((unsigned)u) << 16); }
__device__ __forceinline__ float bflo(unsigned u) { return __uint_as_float(u << 16); }
__device__ __forceinline__ float bfhi(unsigned u) { return __uint_as_float(u & 0xffff0000u); }
__device__ __forceinline__ float bfr(float x) { return (float)(__bf16)x; }
__device__ __forceinline__ unsigned short f2bf(float x) {
    __bf16 b = (__bf16)x;
    return __builtin_bit_cast(unsigned short, b);
}
#define WT_STORES 0
template <typename T>
__device__ __forceinline__ void st_out(T* ptr, T v) {
    static_assert(sizeof(T) == 16, "16-byte stores only");
    *ptr = v;
}
__device__ __forceinline__ unsigned pk2(float a, float b) { return f2bf(a) | ((unsigned)f2bf(b) << 16); }

__device__ __forceinline__ void sincos_acc(float x, float& s, float& c) {
    float kf = rintf(x * 0.63661977236758134f);
    float r = fmaf(kf, -1.5707962513e+00f, x);
    r = fmaf(kf, -7.5497894159e-08f, r);
    r = fmaf(kf, -5.3903029534e-15f, r);
    int k = (int)kf;
    float r2 = r * r;
    float sp = fmaf(r2, -1.9515295891e-04f, 8.3321608736e-03f);
    sp = fmaf(sp, r2, -1.6666654611e-01f);
    sp = fmaf(sp * r2, r, r);
    float cp = fmaf(r2, 2.443315711809948e-05f, -1.388731625493765e-03f);
    cp = fmaf(cp, r2, 4.166664568298827e-02f);
    cp = fmaf(cp * r2, r2, fmaf(-0.5f, r2, 1.0f));
    float ss = (k & 1) ? cp : sp;
    float cc = (k & 1) ? sp : cp;
    s = (k & 2) ? -ss : ss;
    c = ((k + 1) & 2) ? -cc : cc;
}

__device__ __forceinline__ void chan_lam(float lre, float lim, float ldt, float& Lr, float& Li) {
    float dt = bfr(expf(ldt));
    float a = dt * lre, b = dt * lim;
    float ea = expf(a);
    float sb, cb;
    sincos_acc(b, sb, cb);
    Lr = ea * cb;
    Li = ea * sb;
}
__device__ __forceinline__ void chan_consts(float lre, float lim, float ldt, float& Lr, float& Li,
                                            float& cr, float& ci) {
    float dt = bfr(expf(ldt));
    float a = dt * lre, b = dt * lim;
    float ea = expf(a);
    float sb, cb;
    sincos_acc(b, sb, cb);
    Lr = ea * cb;
    Li = ea * sb;
    float sh, chh;
    sincos_acc(0.5f * b, sh, chh);
    float nr = expm1f(a) * cb - 2.0f * sh * sh;
    float ni = Li;
    float den = lre * lre + lim * lim;
    float inv = 1.0f / den;
    cr = (nr * lre + ni * lim) * inv;
    ci = (ni * lre - nr * lim) * inv;
}

#define CMUL_R(ar, ai, br, bi) ((ar) * (br) - (ai) * (bi))
#define CMUL_I(ar, ai, br, bi) ((ar) * (bi) + (ai) * (br))

#define K1_GEMM_BLOCKS 192
#define K1_G_BLOCKS 32
#define K1_W2_BLOCKS 34
#define K1_C_BLOCKS 25
#define K1_GRID (K1_GEMM_BLOCKS + K1_G_BLOCKS + K1_W2_BLOCKS + K1_C_BLOCKS)

__device__ __forceinline__ unsigned swz128(unsigned row, unsigned ch) {
    return row * 128u + ((ch ^ ((row >> 1) & 7u)) << 4);
}
__device__ __forceinline__ unsigned off_b(unsigned row, unsigned ch) {
    return 256u * row + 16u * (ch ^ (((row & 3u) << 2) | ((row >> 2) & 3u)));
}

struct K1Params {
    const unsigned short* x;
    const unsigned short* lre;
    const unsigned short* lim;
    const unsigned short* bre;
    const unsigned short* bim;
    const unsigned short* cre;
    const unsigned short* cim;
    const unsigned short* ldt;
    const unsigned short* w1;
    const unsigned short* w2;
    const unsigned short* gw;
    char* ws;
};

__device__ __forceinline__ bf16x4 tr_read_asm(unsigned lds_addr) {
    bf16x4 r;
    asm volatile("ds_read_b64_tr_b16 %0, %1" : "=v"(r) : "v"(lds_addr) : "memory");
    return r;
}
__device__ __forceinline__ bf16x8 lds_read128_asm(unsigned lds_addr) {
    bf16x8 r;
    asm volatile("ds_read_b128 %0, %1" : "=v"(r) : "v"(lds_addr) : "memory");
    return r;
}
__device__ __forceinline__ unsigned lds_addr_of(const char* ptr) {
    return (unsigned)(size_t)(__attribute__((address_space(3))) const char*)ptr;
}
#define K1_STAGE_BYTES 49152
#define K1_CONST_OFF (3 * K1_STAGE_BYTES)
#define RAW_BARRIER() do { asm volatile("s_waitcnt lgkmcnt(0)" ::: "memory"); __builtin_amdgcn_s_barrier(); } while (0)

template <bool SSM>
__device__ __forceinline__ void k1_tile(const K1Params& p, char* smem, int mtile, int ntile) {
    const int tid = threadIdx.x;
    const int lane = tid & 63;
    const int wid = __builtin_amdgcn_readfirstlane(tid >> 6);
    const int t0 = mtile * 256;
    const int wt = wid >> 1, wn = wid & 1;
    char* stamp_base = p.ws;
    STAMP(0, 0);

    const unsigned short* xs_src[4];
#pragma unroll
    for (int i = 0; i < 4; ++i) {
        unsigned row = (4 * wid + i) * 8 + (lane >> 3);
        unsigned chk = (lane & 7) ^ ((row >> 1) & 7);
        xs_src[i] = p.x + (size_t)(t0 + row) * DM + chk * 8;
    }
    const unsigned short* ws_src[2];
#pragma unroll
    for (int i = 0; i < 2; ++i) {
        if (SSM) {
            unsigned row = (2 * wid + i) * 8 + (lane >> 3);
            unsigned chk = (lane & 7) ^ ((row >> 1) & 7);
            unsigned grp = row >> 5;
            const unsigned short* base = (grp & 1) ? p.bim : p.bre;
            ws_src[i] = base + (size_t)(ntile * 64 + (row & 31) + 32 * (grp >> 1)) * DM + chk * 8;
        } else {
            unsigned row = (2 * wid + i) * 4 + (lane >> 4);
            unsigned chk = (lane & 15) ^ (((row & 3u) << 2) | ((row >> 2) & 3u));
            int n = ntile * 128 + chk * 8;
            if (n > FF - 8) n = FF - 8;
            ws_src[i] = p.w1 + (size_t)row * FF + n;
        }
    }
#define K1_GLDS(KT)                                                                                  \
    do {                                                                                             \
        char* st_ = smem + ((KT) % 3) * K1_STAGE_BYTES;                                              \
        _Pragma("unroll") for (int i = 0; i < 4; ++i)                                                \
            __builtin_amdgcn_global_load_lds((const unsigned*)(xs_src[i] + (KT) * 64),               \
                                             (unsigned*)(st_ + (4 * wid + i) * 1024), 16, 0, 0);     \
        _Pragma("unroll") for (int i = 0; i < 2; ++i)                                                \
            __builtin_amdgcn_global_load_lds((const unsigned*)(ws_src[i] + (SSM ? (size_t)(KT) * 64 : (size_t)(KT) * 64 * FF)), \
                                             (unsigned*)(st_ + 32768 + (2 * wid + i) * 1024), 16, 0, 0); \
    } while (0)

    if (SSM) {
        const int cc = ntile * 64 + 2 * (lane & 31);
        __builtin_amdgcn_global_load_lds((const unsigned*)(p.lre + cc), (unsigned*)(smem + K1_CONST_OFF), 4, 0, 0);
        __builtin_amdgcn_global_load_lds((const unsigned*)(p.lim + cc), (unsigned*)(smem + K1_CONST_OFF + 256), 4, 0, 0);
        __builtin_amdgcn_global_load_lds((const unsigned*)(p.ldt + cc), (unsigned*)(smem + K1_CONST_OFF + 512), 4, 0, 0);
    }
    K1_GLDS(0);
    K1_GLDS(1);
    K1_GLDS(2);

    f32x16 acc[2][2];
#pragma unroll
    for (int i = 0; i < 2; ++i)
#pragma unroll
        for (int j = 0; j < 2; ++j)
#pragma unroll
            for (int r = 0; r < 16; ++r) acc[i][j][r] = 0.f;

    const unsigned l31 = lane & 31, lh = lane >> 5;
    const int ch = ntile * 64 + wn * 32 + l31;
    const unsigned xa0 = wt * 64 + l31, xa1 = xa0 + 32;
    const unsigned ba0 = wn * 64 + l31, ba1 = ba0 + 32;
    const unsigned tq = (lane & 15) >> 2, tp = lane & 3, tblk = (lane >> 4) & 1;

#define K1_FRAGS(F, T, XS, WSM, S)                                                                   \
    do {                                                                                             \
        const unsigned xb_ = lds_addr_of(XS);                                                        \
        const unsigned wb_ = lds_addr_of(WSM);                                                       \
        F[0] = lds_read128_asm(xb_ + swz128(xa0, 2 * (S) + lh));                                     \
        F[1] = lds_read128_asm(xb_ + swz128(xa1, 2 * (S) + lh));                                     \
        if (SSM) {                                                                                   \
            F[2] = lds_read128_asm(wb_ + swz128(ba0, 2 * (S) + lh));                                 \
            F[3] = lds_read128_asm(wb_ + swz128(ba1, 2 * (S) + lh));                                 \
        } else {                                                                                     \
            _Pragma("unroll") for (int i = 0; i < 2; ++i) {                                          \
                unsigned c = 2 * wn + i;                                                             \
                unsigned a0 = off_b(16 * (S) + 8 * lh + 0 + tq, 4 * c + 2 * tblk + (tp >> 1)) + 8 * (tp & 1); \
                unsigned a1 = off_b(16 * (S) + 8 * lh + 4 + tq, 4 * c + 2 * tblk + (tp >> 1)) + 8 * (tp & 1); \
                T[2 * i] = tr_read_asm(wb_ + a0);                                                    \
                T[2 * i + 1] = tr_read_asm(wb_ + a1);                                                \
            }                                                                                        \
        }                                                                                            \
    } while (0)
#define K1_TWAIT(F, T, LAST)                                                                         \
    do {                                                                                             \
        if (SSM) {                                                                                   \
            if (LAST) asm volatile("s_waitcnt lgkmcnt(0)" : "+v"(F[0]), "+v"(F[1]), "+v"(F[2]), "+v"(F[3]) :: "memory"); \
            else asm volatile("s_waitcnt lgkmcnt(4)" : "+v"(F[0]), "+v"(F[1]), "+v"(F[2]), "+v"(F[3]) :: "memory"); \
        } else {                                                                                     \
            if (LAST) asm volatile("s_waitcnt lgkmcnt(0)" : "+v"(F[0]), "+v"(F[1]), "+v"(T[0]), "+v"(T[1]), "+v"(T[2]), "+v"(T[3]) :: "memory"); \
            else asm volatile("s_waitcnt lgkmcnt(6)" : "+v"(F[0]), "+v"(F[1]), "+v"(T[0]), "+v"(T[1]), "+v"(T[2]), "+v"(T[3]) :: "memory"); \
            F[2] = __builtin_shufflevector(T[0], T[1], 0, 1, 2, 3, 4, 5, 6, 7);                      \
            F[3] = __builtin_shufflevector(T[2], T[3], 0, 1, 2, 3, 4, 5, 6, 7);                      \
        }                                                                                            \
    } while (0)
#define K1_MMA(F)                                                                                    \
    do {                                                                                             \
        if (SSM) {                                                                                   \
            acc[0][0] = __builtin_amdgcn_mfma_f32_32x32x16_bf16(F[0], F[2], acc[0][0], 0, 0, 0);     \
            acc[0][1] = __builtin_amdgcn_mfma_f32_32x32x16_bf16(F[0], F[3], acc[0][1], 0, 0, 0);     \
            acc[1][0] = __builtin_amdgcn_mfma_f32_32x32x16_bf16(F[1], F[2], acc[1][0], 0, 0, 0);     \
            acc[1][1] = __builtin_amdgcn_mfma_f32_32x32x16_bf16(F[1], F[3], acc[1][1], 0, 0, 0);     \
        } else {                                                                                     \
            acc[0][0] = __builtin_amdgcn_mfma_f32_32x32x16_bf16(F[2], F[0], acc[0][0], 0, 0, 0);     \
            acc[0][1] = __builtin_amdgcn_mfma_f32_32x32x16_bf16(F[2], F[1], acc[0][1], 0, 0, 0);     \
            acc[1][0] = __builtin_amdgcn_mfma_f32_32x32x16_bf16(F[3], F[0], acc[1][0], 0, 0, 0);     \
            acc[1][1] = __builtin_amdgcn_mfma_f32_32x32x16_bf16(F[3], F[1], acc[1][1], 0, 0, 0);     \
        }                                                                                            \
    } while (0)
    bf16x8 fA[4], fB[4];
    bf16x4 tA[4], tB[4];
#define K1_STEP(KT)                                                                                  \
    do {                                                                                             \
        char* xs = smem + ((KT) % 3) * K1_STAGE_BYTES;                                               \
        char* wsm = xs + 32768;                                                                      \
        char* xn = smem + (((KT) + 1) % 3) * K1_STAGE_BYTES;                                         \
        char* wn_ = xn + 32768;                                                                      \
        K1_TWAIT(fA, tA, 0);                                                                         \
        K1_MMA(fA);                                                                                  \
        __builtin_amdgcn_sched_barrier(0);                                                           \
        K1_FRAGS(fA, tA, xs, wsm, 2);                                                                \
        __builtin_amdgcn_sched_barrier(0);                                                           \
        K1_TWAIT(fB, tB, 0);                                                                         \
        K1_MMA(fB);                                                                                  \
        __builtin_amdgcn_sched_barrier(0);                                                           \
        K1_FRAGS(fB, tB, xs, wsm, 3);                                                                \
        __builtin_amdgcn_sched_barrier(0);                                                           \
        K1_TWAIT(fA, tA, 0);                                                                         \
        K1_MMA(fA);                                                                                  \
        __builtin_amdgcn_sched_barrier(0);                                                           \
        if ((KT) + 1 < 10) {                                                                         \
            if ((KT) + 2 < 10) asm volatile("s_waitcnt vmcnt(6)" ::: "memory");                      \
            else asm volatile("s_waitcnt vmcnt(0)" ::: "memory");                                    \
            RAW_BARRIER();                                                                           \
            if ((KT) + 3 < 10) K1_GLDS((KT) + 3);                                                    \
            K1_FRAGS(fA, tA, xn, wn_, 0);                                                            \
            __builtin_amdgcn_sched_barrier(0);                                                       \
            K1_TWAIT(fB, tB, 0);                                                                     \
        } else {                                                                                     \
            K1_TWAIT(fB, tB, 1);                                                                     \
        }                                                                                            \
        K1_MMA(fB);                                                                                  \
        __builtin_amdgcn_sched_barrier(0);                                                           \
        if ((KT) + 1 < 10) K1_FRAGS(fB, tB, xn, wn_, 1);                                             \
        __builtin_amdgcn_sched_barrier(0);                                                           \
        STAMP(0, 8 + (KT));                                                                          \
    } while (0)
    asm volatile("s_waitcnt vmcnt(12)" ::: "memory");
    RAW_BARRIER();
    K1_FRAGS(fA, tA, smem, smem + 32768, 0);
    K1_FRAGS(fB, tB, smem, smem + 32768, 1);
    __builtin_amdgcn_sched_barrier(0);
    float Lr = 0.f, Li = 0.f, cr = 0.f, ci = 0.f, L2r = 0.f, L2i = 0.f, L3r = 0.f, L3i = 0.f, L4r = 0.f, L4i = 0.f;
    float Pr = 0.f, Pi = 0.f;
    if (SSM) {
        const unsigned short* cst = (const unsigned short*)(smem + K1_CONST_OFF);
        const int cl0 = wn * 32 + l31;
        chan_consts(bf2f(cst[cl0]), bf2f(cst[128 + cl0]), bf2f(cst[256 + cl0]), Lr, Li, cr, ci);
        L2r = CMUL_R(Lr, Li, Lr, Li); L2i = CMUL_I(Lr, Li, Lr, Li);
        L3r = CMUL_R(L2r, L2i, Lr, Li); L3i = CMUL_I(L2r, L2i, Lr, Li);
        L4r = CMUL_R(L2r, L2i, L2r, L2i); L4i = CMUL_I(L2r, L2i, L2r, L2i);
        Pr = L4r; Pi = L4i;
#pragma unroll
        for (int i = 0; i < 4; ++i) { float nr = Pr * Pr - Pi * Pi, ni = 2.f * Pr * Pi; Pr = nr; Pi = ni; }
    }
    __builtin_amdgcn_sched_barrier(0);
    K1_STEP(0);
    K1_STEP(1);
    K1_STEP(2);
    K1_STEP(3);
    K1_STEP(4);
    K1_STEP(5);
    K1_STEP(6);
    K1_STEP(7);
    K1_STEP(8);
    K1_STEP(9);
#undef K1_STEP
#undef K1_FRAGS
#undef K1_TWAIT
#undef K1_MMA
#undef K1_GLDS
    STAMP(0, SSM ? 1 : 2);

    if (SSM) {
        f32x16 vr[2], vi[2];
#pragma unroll
        for (int mi = 0; mi < 2; ++mi)
#pragma unroll
            for (int r = 0; r < 16; ++r) {
                float ar = acc[mi][0][r], ai = acc[mi][1][r];
                vr[mi][r] = cr * ar - ci * ai;
                vi[mi][r] = cr * ai + ci * ar;
            }
#pragma unroll
        for (int mi = 0; mi < 2; ++mi)
#pragma unroll
            for (int j = 0; j < 4; ++j)
#pragma unroll
                for (int i = 1; i < 4; ++i) {
                    int b = 4 * j + i;
                    float pr = vr[mi][b - 1], pi = vi[mi][b - 1];
                    vr[mi][b] += Lr * pr - Li * pi;
                    vi[mi][b] += Lr * pi + Li * pr;
                }
        float E0r[8], E0i[8], E1r[8], E1i[8];
#pragma unroll
        for (int g = 0; g < 8; ++g) {
            unsigned er = __float_as_uint(vr[g >> 2][4 * (g & 3) + 3]), ei = __float_as_uint(vi[g >> 2][4 * (g & 3) + 3]);
            auto sr = __builtin_amdgcn_permlane32_swap(er, er, false, false);
            auto si = __builtin_amdgcn_permlane32_swap(ei, ei, false, false);
            E0r[g] = __uint_as_float(sr[0]); E0i[g] = __uint_as_float(si[0]);
            E1r[g] = __uint_as_float(sr[1]); E1i[g] = __uint_as_float(si[1]);
        }
        float aR = 0.f, aI = 0.f;
#pragma unroll
        for (int g = 0; g < 8; ++g) {
            float c1r = L4r * aR - L4i * aI + E0r[g], c1i = L4r * aI + L4i * aR + E0i[g];
            aR = L4r * c1r - L4i * c1i + E1r[g];
            aI = L4r * c1i + L4i * c1r + E1i[g];
        }

        f32x2* aggL = (f32x2*)smem;
        f32x2* hinL = (f32x2*)(smem + 2048);
        const int cl = wn * 32 + l31;
        STAMP(0, 24);
        __syncthreads();
        if (lh == 0) aggL[wt * 64 + cl] = (f32x2){aR, aI};
        __syncthreads();
        STAMP(0, 25);
        float bR = 0.f, bI = 0.f, inR = 0.f, inI = 0.f;
#pragma unroll
        for (int w = 0; w < 4; ++w) {
            if (w == wt) { inR = bR; inI = bI; }
            f32x2 a = aggL[w * 64 + cl];
            float nr = Pr * bR - Pi * bI + a[0], ni = Pr * bI + Pi * bR + a[1];
            bR = nr; bI = ni;
        }
        if (wt == 0) {
            unsigned long long* gran = (unsigned long long*)(p.ws + WS_GRAN);
            const float myv = lh ? bI : bR;
            __hip_atomic_store(&gran[((size_t)mtile * PN + ch) * 2 + lh],
                               (1ull << 32) | (unsigned long long)__float_as_uint(myv), __ATOMIC_RELAXED,
                               __HIP_MEMORY_SCOPE_AGENT);
            float Qr = Pr * Pr - Pi * Pi, Qi = 2.f * Pr * Pi;
            { float nr = Qr * Qr - Qi * Qi, ni = 2.f * Qr * Qi; Qr = nr; Qi = ni; }
            unsigned val[7];
            for (unsigned spin = 0; spin < (1u << 24); ++spin) {
                bool ok = true;
#pragma unroll
                for (int m = 0; m < 7; ++m) {
                    if (m < mtile) {
                        unsigned long long x = __hip_atomic_load(&gran[((size_t)m * PN + ch) * 2 + lh], __ATOMIC_RELAXED,
                                                                 __HIP_MEMORY_SCOPE_AGENT);
                        val[m] = (unsigned)x;
                        ok = ok && ((x >> 32) == 1ull);
                    }
                }
                if (__all(ok)) break;
                __builtin_amdgcn_s_sleep(1);
            }
            STAMP(0, 26);
            float hR = 0.f, hI = 0.f;
#pragma unroll
            for (int m = 0; m < 7; ++m) {
                if (m < mtile) {
                    auto sw = __builtin_amdgcn_permlane32_swap(val[m], val[m], false, false);
                    float er = __uint_as_float(sw[0]), ei = __uint_as_float(sw[1]);
                    float nr = Qr * hR - Qi * hI + er, ni = Qr * hI + Qi * hR + ei;
                    hR = nr; hI = ni;
                }
            }
            if (lh == 0) hinL[cl] = (f32x2){hR, hI};
        }
        __syncthreads();
        STAMP(0, 27);
        float cR, cI;
        {
            f32x2 hb = hinL[cl];
            float hR = hb[0], hI = hb[1];
#pragma unroll
            for (int w = 0; w < 3; ++w)
                if (w < wt) { float nr = Pr * hR - Pi * hI, ni = Pr * hI + Pi * hR; hR = nr; hI = ni; }
            cR = hR + inR; cI = hI + inI;
        }
#pragma unroll
        for (int g = 0; g < 8; ++g) {
            const int mi = g >> 2, j = g & 3;
            float c0r = cR, c0i = cI;
            float c1r = L4r * cR - L4i * cI + E0r[g];
            float c1i = L4r * cI + L4i * cR + E0i[g];
            cR = L4r * c1r - L4i * c1i + E1r[g];
            cI = L4r * c1i + L4i * c1r + E1i[g];
            float inr = lh ? c1r : c0r, ini = lh ? c1i : c0i;
            vr[mi][4 * j + 0] += Lr * inr - Li * ini;
            vi[mi][4 * j + 0] += Lr * ini + Li * inr;
            vr[mi][4 * j + 1] += L2r * inr - L2i * ini;
            vi[mi][4 * j + 1] += L2r * ini + L2i * inr;
            vr[mi][4 * j + 2] += L3r * inr - L3i * ini;
            vi[mi][4 * j + 2] += L3r * ini + L3i * inr;
            vr[mi][4 * j + 3] += L4r * inr - L4i * ini;
            vi[mi][4 * j + 3] += L4r * ini + L4i * inr;
        }
        STAMP(0, 3);
        char* tile = smem + 4096 + wid * 9216;
#pragma unroll
        for (int mi = 0; mi < 2; ++mi)
#pragma unroll
            for (int r = 0; r < 16; ++r) {
                int tl = mi * 32 + (r & 3) + 8 * (r >> 2) + 4 * lh;
                *(unsigned*)(tile + tl * 144 + l31 * 4) = pk2(vr[mi][r], vi[mi][r]);
            }
        __builtin_amdgcn_fence(__ATOMIC_RELEASE, "wavefront");
        __builtin_amdgcn_wave_barrier();
        __builtin_amdgcn_fence(__ATOMIC_ACQUIRE, "wavefront");
        u32x4* ha = (u32x4*)(p.ws + WS_HA);
#pragma unroll
        for (int mq = 0; mq < 4; ++mq)
#pragma unroll
            for (int k2 = 0; k2 < 2; ++k2) {
                u32x4 v = *(const u32x4*)(tile + (mq * 16 + (lane & 15)) * 144 + (k2 * 4 + (lane >> 4)) * 16);
                int mt = ((t0 + wt * 64) >> 4) + mq;
                int ks = ((ntile * 64 + wn * 32) >> 4) + k2;
                ha[((size_t)mt * KS_SSM + ks) * 64 + lane] = v;
            }
        STAMP_DRAIN(0, 4);
    } else {
        u32x4* hid = (u32x4*)(p.ws + WS_HIDA);
#pragma unroll
        for (int fi = 0; fi < 2; ++fi) {
            int f0 = ntile * 128 + wn * 64 + fi * 32;
            if (f0 < FF) {
                int ks = f0 >> 5;
#pragma unroll
                for (int mi = 0; mi < 2; ++mi) {
                    int t = t0 + wt * 64 + mi * 32 + l31;
                    int mt = t >> 4;
#pragma unroll
                    for (int half = 0; half < 2; ++half) {
                        u32x4 pk;
#pragma unroll
                        for (int j = 0; j < 4; ++j) {
                            float v0 = acc[fi][mi][8 * half + 2 * j];
                            float v1 = acc[fi][mi][8 * half + 2 * j + 1];
                            v0 = v0 > 0.f ? v0 : 0.f;
                            v1 = v1 > 0.f ? v1 : 0.f;
                            pk[j] = pk2(v0, v1);
                        }
                        int q = 2 * half + lh;
                        st_out(&hid[((size_t)mt * KS_MLP + ks) * 64 + (t & 15) + 16 * q], pk);
                    }
                }
            }
        }
        STAMP_DRAIN(0, 5);
    }
}

__global__ __launch_bounds__(512, 2) void k1_kernel(K1Params p) {
    __shared__ __attribute__((aligned(16))) char smem[3 * K1_STAGE_BYTES + 768];
    const int tid = threadIdx.x;
    const int lane = tid & 63;
    const int wid = tid >> 6;
    int bid = blockIdx.x;

    if (bid < K1_GEMM_BLOCKS) {
        int xcd = bid & 7, idx = bid >> 3;
        int nt24 = (xcd & 3) * 6 + idx / 4, mtile = (xcd >> 2) * 4 + (idx & 3);
        if (nt24 < 10) k1_tile<true>(p, smem, mtile, nt24);
        else k1_tile<false>(p, smem, mtile, nt24 - 10);
        return;
    }
    bid -= K1_GEMM_BLOCKS;
    char* stamp_base = p.ws;
    STAMP(0, 20);
    if (bid < K1_G_BLOCKS) {
        float* g = (float*)(p.ws + WS_G);
        const int l16 = lane & 15;
        u32x4 xv[2][5], gv[5];
#pragma unroll
        for (int i = 0; i < 5; ++i) gv[i] = *(const u32x4*)(p.gw + (l16 + 16 * i) * 8);
#pragma unroll
        for (int u = 0; u < 2; ++u) {
            const int t = ((bid * 8 + wid) * 2 + u) * 4 + (lane >> 4);
#pragma unroll
            for (int i = 0; i < 5; ++i) xv[u][i] = *(const u32x4*)(p.x + (size_t)t * DM + (l16 + 16 * i) * 8);
        }
#pragma unroll
        for (int u = 0; u < 2; ++u) {
            const int t = ((bid * 8 + wid) * 2 + u) * 4 + (lane >> 4);
            float sum = 0.f;
#pragma unroll
            for (int i = 0; i < 5; ++i)
#pragma unroll
                for (int e = 0; e < 4; ++e) {
                    sum += bfr(bflo(xv[u][i][e]) * bflo(gv[i][e]));
                    sum += bfr(bfhi(xv[u][i][e]) * bfhi(gv[i][e]));
                }
#pragma unroll
            for (int o = 8; o > 0; o >>= 1) sum += __shfl_xor(sum, o);
            if (l16 == 0) {
                float z = bfr(sum);
                g[t] = bfr(1.0f / (1.0f + expf(-z)));
            }
        }
        STAMP_DRAIN(0, 21);
        return;
    }
    bid -= K1_G_BLOCKS;
    if (bid < K1_W2_BLOCKS) {
        u32x4* w2t = (u32x4*)(p.ws + WS_W2T);
        char* img = smem + wid * 8192;
        u32x4 v[2][4];
#pragma unroll
        for (int u = 0; u < 2; ++u) {
            int task = (bid * 8 + wid) * 2 + u;
            if (task > KS_MLP * 10 - 1) task = KS_MLP * 10 - 1;
            int ks = task / 10, ng = task % 10;
#pragma unroll
            for (int i = 0; i < 4; ++i) {
                int idx = lane + 64 * i, row = idx >> 3, c8 = idx & 7;
                v[u][i] = *(const u32x4*)(p.w2 + (size_t)(32 * ks + row) * DM + ng * 64 + c8 * 8);
            }
        }
#pragma unroll
        for (int u = 0; u < 2; ++u)
#pragma unroll
            for (int i = 0; i < 4; ++i) {
                int idx = lane + 64 * i, row = idx >> 3, c8 = idx & 7;
                *(u32x4*)(img + u * 4096 + row * 128 + c8 * 16) = v[u][i];
            }
        __builtin_amdgcn_fence(__ATOMIC_RELEASE, "wavefront");
        __builtin_amdgcn_wave_barrier();
        __builtin_amdgcn_fence(__ATOMIC_ACQUIRE, "wavefront");
        const int q = lane >> 4, qq = (lane & 15) >> 2, pp = lane & 3;
#pragma unroll
        for (int u = 0; u < 2; ++u) {
            int task = (bid * 8 + wid) * 2 + u;
            if (task < KS_MLP * 10) {
                int ks = task / 10, ng = task % 10;
#pragma unroll
                for (int c = 0; c < 4; ++c) {
                    int r0 = 16 * (q >> 1) + 4 * (q & 1);
                    unsigned a0 = u * 4096 + (r0 + qq) * 128 + c * 32 + pp * 8;
                    unsigned a1 = a0 + 8 * 128;
                    bf16x4 lo = __builtin_amdgcn_ds_read_tr16_b64_v4bf16((lds_bf16x4_ptr)(img + a0));
                    bf16x4 hi = __builtin_amdgcn_ds_read_tr16_b64_v4bf16((lds_bf16x4_ptr)(img + a1));
                    bf16x8 f = __builtin_shufflevector(lo, hi, 0, 1, 2, 3, 4, 5, 6, 7);
                    w2t[((size_t)(ng * 4 + c) * KS_MLP + ks) * 64 + lane] = __builtin_bit_cast(u32x4, f);
                }
            }
        }
        STAMP_DRAIN(0, 22);
        return;
    }
    bid -= K1_W2_BLOCKS;
    {
        u32x4* ct = (u32x4*)(p.ws + WS_CT);
        u32x4 re[4], im[4];
#pragma unroll
        for (int u = 0; u < 4; ++u) {
            int task = (bid * 8 + wid) * 4 + u;
            int nt = task / 20, pg = task % 20;
            int d = nt * 16 + (lane & 15), pc = pg * 32 + (lane >> 4) * 8;
            re[u] = *(const u32x4*)(p.cre + (size_t)d * PN + pc);
            im[u] = *(const u32x4*)(p.cim + (size_t)d * PN + pc);
        }
#pragma unroll
        for (int u = 0; u < 4; ++u) {
            int task = (bid * 8 + wid) * 4 + u;
            int nt = task / 20, pg = task % 20;
            int c = lane >> 4;
            int ks = pg * 2 + (c >> 1), q0 = 2 * (c & 1);
            u32x4 pa, pb;
            pa[0] = pk2(2.0f * bflo(re[u][0]), -2.0f * bflo(im[u][0]));
            pa[1] = pk2(2.0f * bfhi(re[u][0]), -2.0f * bfhi(im[u][0]));
            pa[2] = pk2(2.0f * bflo(re[u][1]), -2.0f * bflo(im[u][1]));
            pa[3] = pk2(2.0f * bfhi(re[u][1]), -2.0f * bfhi(im[u][1]));
            pb[0] = pk2(2.0f * bflo(re[u][2]), -2.0f * bflo(im[u][2]));
            pb[1] = pk2(2.0f * bfhi(re[u][2]), -2.0f * bfhi(im[u][2]));
            pb[2] = pk2(2.0f * bflo(re[u][3]), -2.0f * bflo(im[u][3]));
            pb[3] = pk2(2.0f * bfhi(re[u][3]), -2.0f * bfhi(im[u][3]));
            size_t o = ((size_t)nt * KS_SSM + ks) * 64 + (lane & 15);
            ct[o + 16 * q0] = pa;
            ct[o + 16 * (q0 + 1)] = pb;
        }
        STAMP_DRAIN(0, 23);
    }
}

struct Frag9 { u32x4 a[4]; u32x4 b[5]; };

__device__ __forceinline__ void g2_load(Frag9& f, const char* __restrict__ ap, const char* __restrict__ bp,
                                        unsigned voff, int KS, int ks) {
#pragma unroll
    for (int i = 0; i < 4; ++i) f.a[i] = *(const u32x4*)(ap + (size_t)((i * KS + ks) * 1024) + voff);
#pragma unroll
    for (int j = 0; j < 5; ++j) f.b[j] = *(const u32x4*)(bp + (size_t)((j * KS + ks) * 1024) + voff);
    __builtin_amdgcn_sched_barrier(0);
}
__device__ __forceinline__ void g2_mma(const Frag9& f, f32x4 (&acc)[4][5]) {
#pragma unroll
    for (int i = 0; i < 4; ++i)
#pragma unroll
        for (int j = 0; j < 5; ++j)
            acc[i][j] = __builtin_amdgcn_mfma_f32_16x16x32_bf16(__builtin_bit_cast(bf16x8, f.b[j]),
                                                                __builtin_bit_cast(bf16x8, f.a[i]), acc[i][j], 0, 0, 0);
    __builtin_amdgcn_sched_barrier(0);
}

template <int KS, int KSTRIDE, int NSTEPS>
__device__ __forceinline__ void g2_run(const char* __restrict__ ap, const char* __restrict__ bp, unsigned voff,
                                       int k0, f32x4 (&acc)[4][5]) {
    Frag9 f0, f1, f2, f3;
    g2_load(f0, ap, bp, voff, KS, k0);
    g2_load(f1, ap, bp, voff, KS, k0 + KSTRIDE);
    g2_load(f2, ap, bp, voff, KS, k0 + 2 * KSTRIDE);
    g2_load(f3, ap, bp, voff, KS, k0 + 3 * KSTRIDE);
#pragma unroll
    for (int st = 0; st < NSTEPS; ++st) {
        const int kn = k0 + (st + 4) * KSTRIDE;
        if ((st & 3) == 0) { g2_mma(f0, acc); if (st + 4 < NSTEPS) g2_load(f0, ap, bp, voff, KS, kn); }
        if ((st & 3) == 1) { g2_mma(f1, acc); if (st + 4 < NSTEPS) g2_load(f1, ap, bp, voff, KS, kn); }
        if ((st & 3) == 2) { g2_mma(f2, acc); if (st + 4 < NSTEPS) g2_load(f2, ap, bp, voff, KS, kn); }
        if ((st & 3) == 3) { g2_mma(f3, acc); if (st + 4 < NSTEPS) g2_load(f3, ap, bp, voff, KS, kn); }
    }
}

struct K2Params {
    const unsigned short* x;
    const unsigned short* dskip;
    char* ws;
    float* out;
};

__global__ __launch_bounds__(512, 2) void k2_kernel(K2Params p) {
    __shared__ __attribute__((aligned(16))) float red[20 * 7 * 256];
    char* stamp_base = p.ws;
    STAMP(1, 0);
    const int bid = blockIdx.x;
    const int xcd = bid & 7, idx = bid >> 3;
    const int mb = (xcd >> 1) * 8 + (idx >> 2), nb = (xcd & 1) * 4 + (idx & 3);
    const int lane = threadIdx.x & 63;
    const int wid = __builtin_amdgcn_readfirstlane(threadIdx.x >> 6);
    const unsigned voff = lane * 16;
    f32x4 acc[4][5];
#pragma unroll
    for (int i = 0; i < 4; ++i)
#pragma unroll
        for (int j = 0; j < 5; ++j) acc[i][j] = (f32x4){0.f, 0.f, 0.f, 0.f};
    if (wid < 5) {
        const char* ap = p.ws + WS_HIDA + (size_t)(mb * 4) * KS_MLP * 1024;
        const char* bp = p.ws + WS_W2T + (size_t)(nb * 5) * KS_MLP * 1024;
        if (wid < 4) g2_run<KS_MLP, 5, 11>(ap, bp, voff, wid, acc);
        else g2_run<KS_MLP, 5, 10>(ap, bp, voff, wid, acc);
    } else {
        const char* ap = p.ws + WS_HA + (size_t)(mb * 4) * KS_SSM * 1024;
        const char* bp = p.ws + WS_CT + (size_t)(nb * 5) * KS_SSM * 1024;
        if (wid == 5) g2_run<KS_SSM, 3, 14>(ap, bp, voff, 0, acc);
        else g2_run<KS_SSM, 3, 13>(ap, bp, voff, wid - 5, acc);
    }
    STAMP(1, 2);
    const float* g = (const float*)(p.ws + WS_G);
    u32x2 ex[3], ed[3];
    float eg[3];
#pragma unroll
    for (int o = 0; o < 3; ++o) {
        int tau = wid + 8 * o;
        if (tau > 19) tau = 19;
        int i = tau / 5, j = tau % 5;
        int t = mb * 64 + i * 16 + (lane & 15);
        int d = nb * 80 + j * 16 + (lane >> 4) * 4;
        eg[o] = g[t];
        ex[o] = *(const u32x2*)(p.x + (size_t)t * DM + d);
        ed[o] = *(const u32x2*)(p.dskip + d);
    }
    f32x4* r4 = (f32x4*)red;
#pragma unroll
    for (int tau = 0; tau < 20; ++tau) {
        const int owner = tau % 8;
        if (wid != owner) {
            int wslot = wid < owner ? wid : wid - 1;
            r4[(tau * 7 + wslot) * 64 + lane] = acc[tau / 5][tau % 5];
        }
    }
    __syncthreads();
#pragma unroll
    for (int tau = 0; tau < 20; ++tau) {
        if (wid == tau % 8) {
            const int o = tau / 8, owner = tau % 8;
            f32x4 sm = (f32x4){0.f, 0.f, 0.f, 0.f}, ss = (f32x4){0.f, 0.f, 0.f, 0.f};
            if (owner < 5) sm = acc[tau / 5][tau % 5]; else ss = acc[tau / 5][tau % 5];
#pragma unroll
            for (int w = 0; w < 7; ++w) {
                const int writer = w < owner ? w : w + 1;
                f32x4 v = r4[(tau * 7 + w) * 64 + lane];
                if (writer < 5) sm += v; else ss += v;
            }
            const int i = tau / 5, j = tau % 5;
            int t = mb * 64 + i * 16 + (lane & 15);
            int d = nb * 80 + j * 16 + (lane >> 4) * 4;
            const float gg = eg[o];
            const float omg = bfr(1.0f - gg);
            float s0 = bfr(bflo(ex[o][0]) * bflo(ed[o][0]));
            float s1 = bfr(bfhi(ex[o][0]) * bfhi(ed[o][0]));
            float s2 = bfr(bflo(ex[o][1]) * bflo(ed[o][1]));
            float s3 = bfr(bfhi(ex[o][1]) * bfhi(ed[o][1]));
            f32x4 res;
            res[0] = gg * (ss[0] + s0) + bfr(omg * bfr(sm[0]));
            res[1] = gg * (ss[1] + s1) + bfr(omg * bfr(sm[1]));
            res[2] = gg * (ss[2] + s2) + bfr(omg * bfr(sm[2]));
            res[3] = gg * (ss[3] + s3) + bfr(omg * bfr(sm[3]));
            *(f32x4*)(p.out + (size_t)t * DM + d) = res;
        }
    }
    STAMP(1, 3);
    STAMP_DRAIN(1, 4);
}

__global__ __launch_bounds__(512) void k0_zero(u32x4* g) {
    g[blockIdx.x * 512 + threadIdx.x] = (u32x4){0u, 0u, 0u, 0u};
}

extern "C" void kernel_launch(void* const* d_in, const int* in_sizes, int n_in, void* d_out, int out_size,
                              void* d_ws, size_t ws_size, hipStream_t stream) {
    const unsigned short* x = (const unsigned short*)d_in[0];
    const unsigned short* lre = (const unsigned short*)d_in[1];
    const unsigned short* lim = (const unsigned short*)d_in[2];
    const unsigned short* bre = (const unsigned short*)d_in[3];
    const unsigned short* bim = (const unsigned short*)d_in[4];
    const unsigned short* cre = (const unsigned short*)d_in[5];
    const unsigned short* cim = (const unsigned short*)d_in[6];
    const unsigned short* ldt = (const unsigned short*)d_in[7];
    const unsigned short* dskip = (const unsigned short*)d_in[8];
    const unsigned short* w1 = (const unsigned short*)d_in[10];
    const unsigned short* w2 = (const unsigned short*)d_in[11];
    const unsigned short* gw = (const unsigned short*)d_in[12];
    char* ws = (char*)d_ws;
    float* out = (float*)d_out;

    k0_zero<<<(int)(WS_GRAN_SZ / 8192), 512, 0, stream>>>((u32x4*)(ws + WS_GRAN));
    K1Params p1{x, lre, lim, bre, bim, cre, cim, ldt, w1, w2, gw, ws};
    k1_kernel<<<K1_GRID, 512, 0, stream>>>(p1);
    K2Params p2{x, dskip, ws, out};
    k2_kernel<<<256, 512, 0, stream>>>(p2);
}
```

```cpp
#include <hip/hip_runtime.h>
#include <hip/hip_bf16.h>
#include <stdint.h>

typedef __attribute__((ext_vector_type(8))) __bf16 bf16x8;
typedef __attribute__((ext_vector_type(4))) __bf16 bf16x4;
typedef __attribute__((ext_vector_type(16))) float f32x16;
typedef __attribute__((ext_vector_type(4))) float f32x4;
typedef __attribute__((ext_vector_type(2))) float f32x2;
typedef __attribute__((ext_vector_type(4))) unsigned u32x4;
typedef __attribute__((ext_vector_type(2))) unsigned u32x2;
typedef __attribute__((address_space(3))) bf16x4* lds_bf16x4_ptr;

#define LSEQ 2048
#define DM 640
#define PN 640
#define FF 1728
#define CHUNK 64
#define NCHUNK (LSEQ / CHUNK)
#define KS_MLP (FF / 32)
#define KS_SSM (2 * PN / 32)
#define MT (LSEQ / 16)
#define NT (DM / 16)

#define WS_GRAN 0
#define WS_GRAN_SZ ((size_t)16 * PN * 2 * 8)
#define WS_HIDA (WS_GRAN + WS_GRAN_SZ)
#define WS_HIDA_SZ ((size_t)MT * KS_MLP * 1024)
#define WS_HA (WS_HIDA + WS_HIDA_SZ)
#define WS_HA_SZ ((size_t)MT * KS_SSM * 1024)
#define WS_W2T (WS_HA + WS_HA_SZ)
#define WS_W2T_SZ ((size_t)NT * KS_MLP * 1024)
#define WS_CT (WS_W2T + WS_W2T_SZ)
#define WS_CT_SZ ((size_t)NT * KS_SSM * 1024)
#define WS_G (WS_CT + WS_CT_SZ)

#define STAMP(kid, idx) do {} while (0)
#define STAMP_DRAIN(kid, idx) do {} while (0)
#define WSTAMP(kid, idx) do {} while (0)

__device__ __forceinline__ float bf2f(unsigned short u) { return __uint_as_float(((unsigned)u) << 16); }
__device__ __forceinline__ float bflo(unsigned u) { return __uint_as_float(u << 16); }
__device__ __forceinline__ float bfhi(unsigned u) { return __uint_as_float(u & 0xffff0000u); }
__device__ __forceinline__ float bfr(float x) { return (float)(__bf16)x; }
__device__ __forceinline__ unsigned short f2bf(float x) {
    __bf16 b = (__bf16)x;
    return __builtin_bit_cast(unsigned short, b);
}
#define WT_STORES 0
template <typename T>
__device__ __forceinline__ void st_out(T* ptr, T v) {
    static_assert(sizeof(T) == 16, "16-byte stores only");
    *ptr = v;
}
__device__ __forceinline__ unsigned pk2(float a, float b) { return f2bf(a) | ((unsigned)f2bf(b) << 16); }

__device__ __forceinline__ void sincos_acc(float x, float& s, float& c) {
    float kf = rintf(x * 0.63661977236758134f);
    float r = fmaf(kf, -1.5707962513e+00f, x);
    r = fmaf(kf, -7.5497894159e-08f, r);
    r = fmaf(kf, -5.3903029534e-15f, r);
    int k = (int)kf;
    float r2 = r * r;
    float sp = fmaf(r2, -1.9515295891e-04f, 8.3321608736e-03f);
    sp = fmaf(sp, r2, -1.6666654611e-01f);
    sp = fmaf(sp * r2, r, r);
    float cp = fmaf(r2, 2.443315711809948e-05f, -1.388731625493765e-03f);
    cp = fmaf(cp, r2, 4.166664568298827e-02f);
    cp = fmaf(cp * r2, r2, fmaf(-0.5f, r2, 1.0f));
    float ss = (k & 1) ? cp : sp;
    float cc = (k & 1) ? sp : cp;
    s = (k & 2) ? -ss : ss;
    c = ((k + 1) & 2) ? -cc : cc;
}

__device__ __forceinline__ void chan_lam(float lre, float lim, float ldt, float& Lr, float& Li) {
    float dt = bfr(expf(ldt));
    float a = dt * lre, b = dt * lim;
    float ea = expf(a);
    float sb, cb;
    sincos_acc(b, sb, cb);
    Lr = ea * cb;
    Li = ea * sb;
}
__device__ __forceinline__ void chan_consts(float lre, float lim, float ldt, float& Lr, float& Li,
                                            float& cr, float& ci) {
    float dt = bfr(expf(ldt));
    float a = dt * lre, b = dt * lim;
    float ea = expf(a);
    float sb, cb;
    sincos_acc(b, sb, cb);
    Lr = ea * cb;
    Li = ea * sb;
    float sh, chh;
    sincos_acc(0.5f * b, sh, chh);
    float nr = expm1f(a) * cb - 2.0f * sh * sh;
    float ni = Li;
    float den = lre * lre + lim * lim;
    float inv = 1.0f / den;
    cr = (nr * lre + ni * lim) * inv;
    ci = (ni * lre - nr * lim) * inv;
}

#define CMUL_R(ar, ai, br, bi) ((ar) * (br) - (ai) * (bi))
#define CMUL_I(ar, ai, br, bi) ((ar) * (bi) + (ai) * (br))

#define K1_SSM_BLOCKS 160
#define K1_MLP_BLOCKS 88
#define K1_GRID (K1_SSM_BLOCKS + K1_MLP_BLOCKS)
#define K0_Z_BLOCKS 20
#define K0_G_BLOCKS 32
#define K0_W2_BLOCKS 34
#define K0_C_BLOCKS 25
#define K0_GRID (K0_Z_BLOCKS + K0_G_BLOCKS + K0_W2_BLOCKS + K0_C_BLOCKS)

__device__ __forceinline__ unsigned swz128(unsigned row, unsigned ch) {
    return row * 128u + ((ch ^ ((row >> 1) & 7u)) << 4);
}
__device__ __forceinline__ unsigned off_b(unsigned row, unsigned ch) {
    return 256u * row + 16u * (ch ^ (((row & 3u) << 2) | ((row >> 2) & 3u)));
}

struct K1Params {
    const unsigned short* x;
    const unsigned short* lre;
    const unsigned short* lim;
    const unsigned short* bre;
    const unsigned short* bim;
    const unsigned short* cre;
    const unsigned short* cim;
    const unsigned short* ldt;
    const unsigned short* w1;
    const unsigned short* w2;
    const unsigned short* gw;
    char* ws;
};

__device__ __forceinline__ bf16x4 tr_read_asm(unsigned lds_addr) {
    bf16x4 r;
    asm volatile("ds_read_b64_tr_b16 %0, %1" : "=v"(r) : "v"(lds_addr) : "memory");
    return r;
}
__device__ __forceinline__ bf16x8 lds_read128_asm(unsigned lds_addr) {
    bf16x8 r;
    asm volatile("ds_read_b128 %0, %1" : "=v"(r) : "v"(lds_addr) : "memory");
    return r;
}
template <int OFF>
__device__ __forceinline__ bf16x4 tr_read_off(unsigned lds_addr) {
    bf16x4 r;
    asm volatile("ds_read_b64_tr_b16 %0, %1 offset:%2" : "=v"(r) : "v"(lds_addr), "n"(OFF) : "memory");
    return r;
}
__device__ __forceinline__ unsigned opaque_s(unsigned v) {
    asm volatile("" : "+s"(v));
    return v;
}
__device__ __forceinline__ unsigned lds_addr_of(const char* ptr) {
    return (unsigned)(size_t)(__attribute__((address_space(3))) const char*)ptr;
}
#define K1_LDS_BYTES (3 * 53248 + 768)
#define RAW_BARRIER() do { asm volatile("s_waitcnt lgkmcnt(0)" ::: "memory"); __builtin_amdgcn_s_barrier(); } while (0)
#define GLDS16(SRC, DST) __builtin_amdgcn_global_load_lds((const unsigned*)(SRC), (unsigned*)(DST), 16, 0, 0)
#define SCHED_FENCE() __builtin_amdgcn_sched_barrier(0)

#define SS_STAGE 32768
#define SS_CONST_OFF (4 * SS_STAGE)
__device__ __forceinline__ void k1_ssm_tile(const K1Params& p, char* smem, int mtile, int ntile) {
    const int tid = threadIdx.x;
    const int lane = tid & 63;
    const int wid = __builtin_amdgcn_readfirstlane(tid >> 6);
    const int t0 = mtile * 128;
    const int wt = wid >> 1, wn = wid & 1;
    char* stamp_base = p.ws;
    STAMP(0, 0);

    const unsigned short* xs_src[2];
    const unsigned short* ws_src[2];
#pragma unroll
    for (int i = 0; i < 2; ++i) {
        unsigned row = (2 * wid + i) * 8 + (lane >> 3);
        unsigned chk = (lane & 7) ^ ((row >> 1) & 7);
        xs_src[i] = p.x + (size_t)(t0 + row) * DM + chk * 8;
        unsigned grp = row >> 5;
        const unsigned short* base = (grp & 1) ? p.bim : p.bre;
        ws_src[i] = base + (size_t)(ntile * 64 + (row & 31) + 32 * (grp >> 1)) * DM + chk * 8;
    }
#define SS_GLDS(KT)                                                                    \
    do {                                                                               \
        char* st_ = smem + ((KT) & 3) * SS_STAGE;                                      \
        GLDS16(xs_src[0] + (KT) * 64, st_ + (2 * wid) * 1024);                         \
        GLDS16(xs_src[1] + (KT) * 64, st_ + (2 * wid + 1) * 1024);                     \
        GLDS16(ws_src[0] + (KT) * 64, st_ + 16384 + (2 * wid) * 1024);                 \
        GLDS16(ws_src[1] + (KT) * 64, st_ + 16384 + (2 * wid + 1) * 1024);             \
    } while (0)
    {
        const int cc = ntile * 64 + 2 * (lane & 31);
        __builtin_amdgcn_global_load_lds((const unsigned*)(p.lre + cc), (unsigned*)(smem + SS_CONST_OFF), 4, 0, 0);
        __builtin_amdgcn_global_load_lds((const unsigned*)(p.lim + cc), (unsigned*)(smem + SS_CONST_OFF + 256), 4, 0, 0);
        __builtin_amdgcn_global_load_lds((const unsigned*)(p.ldt + cc), (unsigned*)(smem + SS_CONST_OFF + 512), 4, 0, 0);
    }
    SS_GLDS(0);
    SS_GLDS(1);
    SS_GLDS(2);
    SS_GLDS(3);

    f32x16 accR, accI;
#pragma unroll
    for (int r = 0; r < 16; ++r) { accR[r] = 0.f; accI[r] = 0.f; }
    const unsigned l31 = lane & 31, lh = lane >> 5;
    const int ch = ntile * 64 + wn * 32 + l31;
    const unsigned xa0 = wt * 32 + l31;
    const unsigned ba0 = wn * 64 + l31, ba1 = ba0 + 32;
    const unsigned sbase = lds_addr_of(smem);

    unsigned sxo[4], sbo0[4], sbo1[4];
#pragma unroll
    for (int q = 0; q < 4; ++q) {
        sxo[q] = swz128(xa0, 2 * q + lh);
        sbo0[q] = 16384 + swz128(ba0, 2 * q + lh);
        sbo1[q] = 16384 + swz128(ba1, 2 * q + lh);
    }
#define SS_FRAGS(F, KT, S)                                                             \
    do {                                                                               \
        const unsigned xb_ = opaque_s(sbase + ((KT) & 3) * SS_STAGE);                  \
        F[0] = lds_read128_asm(xb_ + sxo[S]);                                          \
        F[1] = lds_read128_asm(xb_ + sbo0[S]);                                         \
        F[2] = lds_read128_asm(xb_ + sbo1[S]);                                         \
    } while (0)
#define SS_WAIT(F, N) asm volatile("s_waitcnt lgkmcnt(" #N ")" : "+v"(F[0]), "+v"(F[1]), "+v"(F[2])::"memory")
#define SS_MMA(F)                                                                      \
    do {                                                                               \
        accR = __builtin_amdgcn_mfma_f32_32x32x16_bf16(F[0], F[1], accR, 0, 0, 0);     \
        accI = __builtin_amdgcn_mfma_f32_32x32x16_bf16(F[0], F[2], accI, 0, 0, 0);     \
    } while (0)
    bf16x8 fA[3], fB[3];
#define SS_STEP(KT)                                                                    \
    do {                                                                               \
        SS_WAIT(fA, 3); SS_MMA(fA); SCHED_FENCE();                                     \
        SS_FRAGS(fA, KT, 2); SCHED_FENCE();                                            \
        SS_WAIT(fB, 3); SS_MMA(fB); SCHED_FENCE();                                     \
        SS_FRAGS(fB, KT, 3); SCHED_FENCE();                                            \
        SS_WAIT(fA, 3); SS_MMA(fA); SCHED_FENCE();                                     \
        if ((KT) + 1 < 10) {                                                           \
            if ((KT) + 3 < 10) asm volatile("s_waitcnt vmcnt(8)" ::: "memory");        \
            else if ((KT) + 2 < 10) asm volatile("s_waitcnt vmcnt(4)" ::: "memory");   \
            else asm volatile("s_waitcnt vmcnt(0)" ::: "memory");                      \
            RAW_BARRIER();                                                             \
            if ((KT) + 4 < 10) SS_GLDS((KT) + 4);                                      \
            SS_FRAGS(fA, (KT) + 1, 0); SCHED_FENCE();                                  \
            SS_WAIT(fB, 3);                                                            \
        } else {                                                                       \
            SS_WAIT(fB, 0);                                                            \
        }                                                                              \
        SS_MMA(fB); SCHED_FENCE();                                                     \
        if ((KT) + 1 < 10) SS_FRAGS(fB, (KT) + 1, 1);                                  \
        SCHED_FENCE();                                                                 \
    } while (0)
    asm volatile("s_waitcnt vmcnt(12)" ::: "memory");
    RAW_BARRIER();
    SS_FRAGS(fA, 0, 0);
    SS_FRAGS(fB, 0, 1);
    SCHED_FENCE();
    float Lr, Li, cr, ci;
    {
        const unsigned short* cst = (const unsigned short*)(smem + SS_CONST_OFF);
        const int cl0 = wn * 32 + l31;
        chan_consts(bf2f(cst[cl0]), bf2f(cst[128 + cl0]), bf2f(cst[256 + cl0]), Lr, Li, cr, ci);
    }
    const float L2r = CMUL_R(Lr, Li, Lr, Li), L2i = CMUL_I(Lr, Li, Lr, Li);
    const float L3r = CMUL_R(L2r, L2i, Lr, Li), L3i = CMUL_I(L2r, L2i, Lr, Li);
    const float L4r = CMUL_R(L2r, L2i, L2r, L2i), L4i = CMUL_I(L2r, L2i, L2r, L2i);
    float Pr = L4r, Pi = L4i;
#pragma unroll
    for (int i = 0; i < 3; ++i) { float nr = Pr * Pr - Pi * Pi, ni = 2.f * Pr * Pi; Pr = nr; Pi = ni; }
    SCHED_FENCE();
    SS_STEP(0); SS_STEP(1); SS_STEP(2); SS_STEP(3); SS_STEP(4);
    SS_STEP(5); SS_STEP(6); SS_STEP(7); SS_STEP(8); SS_STEP(9);
#undef SS_STEP
#undef SS_MMA
#undef SS_WAIT
#undef SS_FRAGS
#undef SS_GLDS
    STAMP(0, 1);

    f32x16 vr, vi;
#pragma unroll
    for (int r = 0; r < 16; ++r) {
        vr[r] = cr * accR[r] - ci * accI[r];
        vi[r] = cr * accI[r] + ci * accR[r];
    }
#pragma unroll
    for (int j = 0; j < 4; ++j)
#pragma unroll
        for (int i = 1; i < 4; ++i) {
            int b = 4 * j + i;
            float pr = vr[b - 1], pi = vi[b - 1];
            vr[b] += Lr * pr - Li * pi;
            vi[b] += Lr * pi + Li * pr;
        }
    float E0r[4], E0i[4], E1r[4], E1i[4];
#pragma unroll
    for (int g = 0; g < 4; ++g) {
        unsigned er = __float_as_uint(vr[4 * g + 3]), ei = __float_as_uint(vi[4 * g + 3]);
        auto sr = __builtin_amdgcn_permlane32_swap(er, er, false, false);
        auto si = __builtin_amdgcn_permlane32_swap(ei, ei, false, false);
        E0r[g] = __uint_as_float(sr[0]); E0i[g] = __uint_as_float(si[0]);
        E1r[g] = __uint_as_float(sr[1]); E1i[g] = __uint_as_float(si[1]);
    }
    float aR = 0.f, aI = 0.f;
#pragma unroll
    for (int g = 0; g < 4; ++g) {
        float c1r = L4r * aR - L4i * aI + E0r[g], c1i = L4r * aI + L4i * aR + E0i[g];
        aR = L4r * c1r - L4i * c1i + E1r[g];
        aI = L4r * c1i + L4i * c1r + E1i[g];
    }
    f32x2* aggL = (f32x2*)smem;
    f32x2* hinL = (f32x2*)(smem + 2048);
    const int cl = wn * 32 + l31;
    STAMP(0, 24);
    __syncthreads();
    if (lh == 0) aggL[wt * 64 + cl] = (f32x2){aR, aI};
    __syncthreads();
    STAMP(0, 25);
    float bR = 0.f, bI = 0.f, inR = 0.f, inI = 0.f;
#pragma unroll
    for (int w = 0; w < 4; ++w) {
        if (w == wt) { inR = bR; inI = bI; }
        f32x2 a = aggL[w * 64 + cl];
        float nr = Pr * bR - Pi * bI + a[0], ni = Pr * bI + Pi * bR + a[1];
        bR = nr; bI = ni;
    }
    if (wt == 0) {
        unsigned long long* gran = (unsigned long long*)(p.ws + WS_GRAN);
        const float myv = lh ? bI : bR;
        __hip_atomic_store(&gran[((size_t)mtile * PN + ch) * 2 + lh],
                           (1ull << 32) | (unsigned long long)__float_as_uint(myv), __ATOMIC_RELAXED,
                           __HIP_MEMORY_SCOPE_AGENT);
        float Qr = Pr * Pr - Pi * Pi, Qi = 2.f * Pr * Pi;
        { float nr = Qr * Qr - Qi * Qi, ni = 2.f * Qr * Qi; Qr = nr; Qi = ni; }
        unsigned val[15];
        for (unsigned spin = 0; spin < (1u << 24); ++spin) {
            bool ok = true;
#pragma unroll
            for (int m = 0; m < 15; ++m) {
                if (m < mtile) {
                    unsigned long long x = __hip_atomic_load(&gran[((size_t)m * PN + ch) * 2 + lh], __ATOMIC_RELAXED,
                                                             __HIP_MEMORY_SCOPE_AGENT);
                    val[m] = (unsigned)x;
                    ok = ok && ((x >> 32) == 1ull);
                }
            }
            if (__all(ok)) break;
            __builtin_amdgcn_s_sleep(1);
        }
        STAMP(0, 26);
        float hR = 0.f, hI = 0.f;
#pragma unroll
        for (int m = 0; m < 15; ++m) {
            if (m < mtile) {
                auto sw = __builtin_amdgcn_permlane32_swap(val[m], val[m], false, false);
                float er = __uint_as_float(sw[0]), ei = __uint_as_float(sw[1]);
                float nr = Qr * hR - Qi * hI + er, ni = Qr * hI + Qi * hR + ei;
                hR = nr; hI = ni;
            }
        }
        if (lh == 0) hinL[cl] = (f32x2){hR, hI};
    }
    __syncthreads();
    STAMP(0, 27);
    float cR, cI;
    {
        f32x2 hb = hinL[cl];
        float hR = hb[0], hI = hb[1];
#pragma unroll
        for (int w = 0; w < 3; ++w)
            if (w < wt) { float nr = Pr * hR - Pi * hI, ni = Pr * hI + Pi * hR; hR = nr; hI = ni; }
        cR = hR + inR; cI = hI + inI;
    }
#pragma unroll
    for (int j = 0; j < 4; ++j) {
        float c0r = cR, c0i = cI;
        float c1r = L4r * cR - L4i * cI + E0r[j];
        float c1i = L4r * cI + L4i * cR + E0i[j];
        cR = L4r * c1r - L4i * c1i + E1r[j];
        cI = L4r * c1i + L4i * c1r + E1i[j];
        float inr = lh ? c1r : c0r, ini = lh ? c1i : c0i;
        vr[4 * j + 0] += Lr * inr - Li * ini;
        vi[4 * j + 0] += Lr * ini + Li * inr;
        vr[4 * j + 1] += L2r * inr - L2i * ini;
        vi[4 * j + 1] += L2r * ini + L2i * inr;
        vr[4 * j + 2] += L3r * inr - L3i * ini;
        vi[4 * j + 2] += L3r * ini + L3i * inr;
        vr[4 * j + 3] += L4r * inr - L4i * ini;
        vi[4 * j + 3] += L4r * ini + L4i * inr;
    }
    STAMP(0, 3);
    char* tile = smem + 4096 + wid * 4608;
#pragma unroll
    for (int r = 0; r < 16; ++r) {
        int tl = (r & 3) + 8 * (r >> 2) + 4 * lh;
        *(unsigned*)(tile + tl * 144 + l31 * 4) = pk2(vr[r], vi[r]);
    }
    __builtin_amdgcn_fence(__ATOMIC_RELEASE, "wavefront");
    __builtin_amdgcn_wave_barrier();
    __builtin_amdgcn_fence(__ATOMIC_ACQUIRE, "wavefront");
    u32x4* ha = (u32x4*)(p.ws + WS_HA);
#pragma unroll
    for (int mq = 0; mq < 2; ++mq)
#pragma unroll
        for (int k2 = 0; k2 < 2; ++k2) {
            u32x4 v = *(const u32x4*)(tile + (mq * 16 + (lane & 15)) * 144 + (k2 * 4 + (lane >> 4)) * 16);
            int mt = ((t0 + wt * 32) >> 4) + mq;
            int ks = ((ntile * 64 + wn * 32) >> 4) + k2;
            ha[((size_t)mt * KS_SSM + ks) * 64 + lane] = v;
        }
    STAMP_DRAIN(0, 4);
}

#define ML_STAGE 53248
__device__ __forceinline__ void k1_mlp_tile(const K1Params& p, char* smem, int mtile, int ntile) {
    const int tid = threadIdx.x;
    const int lane = tid & 63;
    const int wid = __builtin_amdgcn_readfirstlane(tid >> 6);
    const int t0 = mtile * 256;
    char* stamp_base = p.ws;
    STAMP(0, 0);

    const unsigned short* xs_src[4];
#pragma unroll
    for (int i = 0; i < 4; ++i) {
        unsigned row = (4 * wid + i) * 8 + (lane >> 3);
        unsigned chk = (lane & 7) ^ ((row >> 1) & 7);
        xs_src[i] = p.x + (size_t)(t0 + row) * DM + chk * 8;
    }
    const unsigned short* wa_src[2];
#pragma unroll
    for (int i = 0; i < 2; ++i) {
        unsigned row = (2 * wid + i) * 4 + (lane >> 4);
        unsigned chk = (lane & 15) ^ (((row & 3u) << 2) | ((row >> 2) & 3u));
        wa_src[i] = p.w1 + (size_t)row * FF + ntile * 160 + chk * 8;
    }
    const unsigned short* wb_src;
    {
        unsigned row = (wid & 3) * 16 + (lane >> 2);
        int n = ntile * 160 + 128 + (lane & 3) * 8;
        if (n > FF - 8) n = FF - 8;
        wb_src = p.w1 + (size_t)row * FF + n;
    }
#define ML_GLDS(KT)                                                                          \
    do {                                                                                     \
        char* st_ = smem + ((KT) % 3) * ML_STAGE;                                            \
        _Pragma("unroll") for (int i = 0; i < 4; ++i) GLDS16(xs_src[i] + (KT) * 64, st_ + (4 * wid + i) * 1024); \
        _Pragma("unroll") for (int i = 0; i < 2; ++i) GLDS16(wa_src[i] + (size_t)(KT) * 64 * FF, st_ + 32768 + (2 * wid + i) * 1024); \
        GLDS16(wb_src + (size_t)(KT) * 64 * FF, st_ + 49152 + (wid & 3) * 1024);             \
    } while (0)
    ML_GLDS(0);
    ML_GLDS(1);
    ML_GLDS(2);

    f32x16 acc[5];
#pragma unroll
    for (int i = 0; i < 5; ++i)
#pragma unroll
        for (int r = 0; r < 16; ++r) acc[i][r] = 0.f;
    const unsigned l31 = lane & 31, lh = lane >> 5;
    const unsigned xa0 = wid * 32 + l31;
    const unsigned tq = (lane & 15) >> 2, tp = lane & 3, tblk = (lane >> 4) & 1;
    const unsigned sbase = lds_addr_of(smem);

    unsigned mxo[4], mwo[8], mbo;
#pragma unroll
    for (int q = 0; q < 4; ++q) mxo[q] = swz128(xa0, 2 * q + lh);
#pragma unroll
    for (int c = 0; c < 4; ++c) {
        mwo[2 * c] = 32768 + off_b(8 * lh + 0 + tq, 4 * c + 2 * tblk + (tp >> 1)) + 8 * (tp & 1);
        mwo[2 * c + 1] = 32768 + off_b(8 * lh + 4 + tq, 4 * c + 2 * tblk + (tp >> 1)) + 8 * (tp & 1);
    }
    mbo = 49152 + (8 * lh + tq) * 64 + tblk * 32 + tp * 8;
#define ML_FRAGS(X, T, KT, S)                                                                \
    do {                                                                                     \
        const unsigned xb_ = opaque_s(sbase + ((KT) % 3) * ML_STAGE);                        \
        X = lds_read128_asm(xb_ + mxo[S]);                                                   \
        _Pragma("unroll") for (int c = 0; c < 8; ++c) T[c] = tr_read_off<4096 * (S)>(xb_ + mwo[c]); \
        T[8] = tr_read_off<1024 * (S)>(xb_ + mbo);                                           \
        T[9] = tr_read_off<1024 * (S) + 256>(xb_ + mbo);                                     \
    } while (0)
#define ML_WAIT(X, T, N)                                                                     \
    asm volatile("s_waitcnt lgkmcnt(" #N ")" : "+v"(X), "+v"(T[0]), "+v"(T[1]), "+v"(T[2]), "+v"(T[3]), "+v"(T[4]), \
                 "+v"(T[5]), "+v"(T[6]), "+v"(T[7]), "+v"(T[8]), "+v"(T[9])::"memory")
#define ML_MMA(X, T)                                                                         \
    do {                                                                                     \
        _Pragma("unroll") for (int c = 0; c < 5; ++c) {                                      \
            bf16x8 wf_ = __builtin_shufflevector(T[2 * c], T[2 * c + 1], 0, 1, 2, 3, 4, 5, 6, 7); \
            acc[c] = __builtin_amdgcn_mfma_f32_32x32x16_bf16(wf_, X, acc[c], 0, 0, 0);       \
        }                                                                                    \
    } while (0)
    bf16x8 xA, xB;
    bf16x4 tA[10], tB[10];
#define ML_STEP(KT)                                                                          \
    do {                                                                                     \
        ML_WAIT(xA, tA, 11); ML_MMA(xA, tA); SCHED_FENCE();                                  \
        ML_FRAGS(xA, tA, KT, 2); SCHED_FENCE();                                              \
        ML_WAIT(xB, tB, 11); ML_MMA(xB, tB); SCHED_FENCE();                                  \
        ML_FRAGS(xB, tB, KT, 3); SCHED_FENCE();                                              \
        ML_WAIT(xA, tA, 11); ML_MMA(xA, tA); SCHED_FENCE();                                  \
        if ((KT) + 1 < 10) {                                                                 \
            if ((KT) + 2 < 10) asm volatile("s_waitcnt vmcnt(7)" ::: "memory");              \
            else asm volatile("s_waitcnt vmcnt(0)" ::: "memory");                            \
            RAW_BARRIER();                                                                   \
            if ((KT) + 3 < 10) ML_GLDS((KT) + 3);                                            \
            ML_FRAGS(xA, tA, (KT) + 1, 0); SCHED_FENCE();                                    \
            ML_WAIT(xB, tB, 11);                                                             \
        } else {                                                                             \
            ML_WAIT(xB, tB, 0);                                                              \
        }                                                                                    \
        ML_MMA(xB, tB); SCHED_FENCE();                                                       \
        if ((KT) + 1 < 10) ML_FRAGS(xB, tB, (KT) + 1, 1);                                    \
        SCHED_FENCE();                                                                       \
    } while (0)
    asm volatile("s_waitcnt vmcnt(14)" ::: "memory");
    RAW_BARRIER();
    ML_FRAGS(xA, tA, 0, 0);
    ML_FRAGS(xB, tB, 0, 1);
    SCHED_FENCE();
    ML_STEP(0); ML_STEP(1); ML_STEP(2); ML_STEP(3); ML_STEP(4);
    ML_STEP(5); ML_STEP(6); ML_STEP(7); ML_STEP(8); ML_STEP(9);
#undef ML_STEP
#undef ML_MMA
#undef ML_WAIT
#undef ML_FRAGS
#undef ML_GLDS
    STAMP(0, 2);
    u32x4* hid = (u32x4*)(p.ws + WS_HIDA);
    const int t = t0 + wid * 32 + l31;
    const int mt = t >> 4;
#pragma unroll
    for (int fi = 0; fi < 5; ++fi) {
        int f0 = ntile * 160 + fi * 32;
        if (f0 < FF) {
            int ks = f0 >> 5;
#pragma unroll
            for (int half = 0; half < 2; ++half) {
                u32x4 pk;
#pragma unroll
                for (int j = 0; j < 4; ++j) {
                    float v0 = acc[fi][8 * half + 2 * j];
                    float v1 = acc[fi][8 * half + 2 * j + 1];
                    v0 = v0 > 0.f ? v0 : 0.f;
                    v1 = v1 > 0.f ? v1 : 0.f;
                    pk[j] = pk2(v0, v1);
                }
                int q = 2 * half + lh;
                hid[((size_t)mt * KS_MLP + ks) * 64 + (t & 15) + 16 * q] = pk;
            }
        }
    }
    STAMP_DRAIN(0, 5);
}

__global__ __launch_bounds__(512, 2) void k1_kernel(K1Params p) {
    __shared__ __attribute__((aligned(16))) char smem[K1_LDS_BYTES];
    int bid = blockIdx.x;
    if (bid < K1_SSM_BLOCKS) {
        int xcd = bid & 7, idx = bid >> 3;
        k1_ssm_tile(p, smem, (xcd >> 1) * 4 + (idx & 3), (xcd & 1) * 5 + (idx >> 2));
    } else {
        bid -= K1_SSM_BLOCKS;
        k1_mlp_tile(p, smem, bid & 7, bid >> 3);
    }
}

__global__ __launch_bounds__(512) void k0_kernel(K1Params p) {
    __shared__ __attribute__((aligned(16))) char smem[65536];
    const int tid = threadIdx.x;
    const int lane = tid & 63;
    const int wid = tid >> 6;
    int bid = blockIdx.x;
    char* stamp_base = p.ws;
    if (bid < K0_Z_BLOCKS) {
        ((u32x4*)(p.ws + WS_GRAN))[bid * 512 + tid] = (u32x4){0u, 0u, 0u, 0u};
        return;
    }
    bid -= K0_Z_BLOCKS;
    if (bid < K0_G_BLOCKS) {
        float* g = (float*)(p.ws + WS_G);
        const int l16 = lane & 15;
        u32x4 xv[2][5], gv[5];
#pragma unroll
        for (int i = 0; i < 5; ++i) gv[i] = *(const u32x4*)(p.gw + (l16 + 16 * i) * 8);
#pragma unroll
        for (int u = 0; u < 2; ++u) {
            const int t = ((bid * 8 + wid) * 2 + u) * 4 + (lane >> 4);
#pragma unroll
            for (int i = 0; i < 5; ++i) xv[u][i] = *(const u32x4*)(p.x + (size_t)t * DM + (l16 + 16 * i) * 8);
        }
#pragma unroll
        for (int u = 0; u < 2; ++u) {
            const int t = ((bid * 8 + wid) * 2 + u) * 4 + (lane >> 4);
            float sum = 0.f;
#pragma unroll
            for (int i = 0; i < 5; ++i)
#pragma unroll
                for (int e = 0; e < 4; ++e) {
                    sum += bfr(bflo(xv[u][i][e]) * bflo(gv[i][e]));
                    sum += bfr(bfhi(xv[u][i][e]) * bfhi(gv[i][e]));
                }
#pragma unroll
            for (int o = 8; o > 0; o >>= 1) sum += __shfl_xor(sum, o);
            if (l16 == 0) {
                float z = bfr(sum);
                g[t] = bfr(1.0f / (1.0f + expf(-z)));
            }
        }

        return;
    }
    bid -= K0_G_BLOCKS;
    if (bid < K0_W2_BLOCKS) {
        u32x4* w2t = (u32x4*)(p.ws + WS_W2T);
        char* img = smem + wid * 8192;
        u32x4 v[2][4];
#pragma unroll
        for (int u = 0; u < 2; ++u) {
            int task = (bid * 8 + wid) * 2 + u;
            if (task > KS_MLP * 10 - 1) task = KS_MLP * 10 - 1;
            int ks = task / 10, ng = task % 10;
#pragma unroll
            for (int i = 0; i < 4; ++i) {
                int idx = lane + 64 * i, row = idx >> 3, c8 = idx & 7;
                v[u][i] = *(const u32x4*)(p.w2 + (size_t)(32 * ks + row) * DM + ng * 64 + c8 * 8);
            }
        }
#pragma unroll
        for (int u = 0; u < 2; ++u)
#pragma unroll
            for (int i = 0; i < 4; ++i) {
                int idx = lane + 64 * i, row = idx >> 3, c8 = idx & 7;
                *(u32x4*)(img + u * 4096 + row * 128 + c8 * 16) = v[u][i];
            }
        __builtin_amdgcn_fence(__ATOMIC_RELEASE, "wavefront");
        __builtin_amdgcn_wave_barrier();
        __builtin_amdgcn_fence(__ATOMIC_ACQUIRE, "wavefront");
        const int q = lane >> 4, qq = (lane & 15) >> 2, pp = lane & 3;
#pragma unroll
        for (int u = 0; u < 2; ++u) {
            int task = (bid * 8 + wid) * 2 + u;
            if (task < KS_MLP * 10) {
                int ks = task / 10, ng = task % 10;
#pragma unroll
                for (int c = 0; c < 4; ++c) {
                    int r0 = 16 * (q >> 1) + 4 * (q & 1);
                    unsigned a0 = u * 4096 + (r0 + qq) * 128 + c * 32 + pp * 8;
                    unsigned a1 = a0 + 8 * 128;
                    bf16x4 lo = __builtin_amdgcn_ds_read_tr16_b64_v4bf16((lds_bf16x4_ptr)(img + a0));
                    bf16x4 hi = __builtin_amdgcn_ds_read_tr16_b64_v4bf16((lds_bf16x4_ptr)(img + a1));
                    bf16x8 f = __builtin_shufflevector(lo, hi, 0, 1, 2, 3, 4, 5, 6, 7);
                    w2t[((size_t)(ng * 4 + c) * KS_MLP + ks) * 64 + lane] = __builtin_bit_cast(u32x4, f);
                }
            }
        }

        return;
    }
    bid -= K0_W2_BLOCKS;
    {
        u32x4* ct = (u32x4*)(p.ws + WS_CT);
        u32x4 re[4], im[4];
#pragma unroll
        for (int u = 0; u < 4; ++u) {
            int task = (bid * 8 + wid) * 4 + u;
            int nt = task / 20, pg = task % 20;
            int d = nt * 16 + (lane & 15), pc = pg * 32 + (lane >> 4) * 8;
            re[u] = *(const u32x4*)(p.cre + (size_t)d * PN + pc);
            im[u] = *(const u32x4*)(p.cim + (size_t)d * PN + pc);
        }
#pragma unroll
        for (int u = 0; u < 4; ++u) {
            int task = (bid * 8 + wid) * 4 + u;
            int nt = task / 20, pg = task % 20;
            int c = lane >> 4;
            int ks = pg * 2 + (c >> 1), q0 = 2 * (c & 1);
            u32x4 pa, pb;
            pa[0] = pk2(2.0f * bflo(re[u][0]), -2.0f * bflo(im[u][0]));
            pa[1] = pk2(2.0f * bfhi(re[u][0]), -2.0f * bfhi(im[u][0]));
            pa[2] = pk2(2.0f * bflo(re[u][1]), -2.0f * bflo(im[u][1]));
            pa[3] = pk2(2.0f * bfhi(re[u][1]), -2.0f * bfhi(im[u][1]));
            pb[0] = pk2(2.0f * bflo(re[u][2]), -2.0f * bflo(im[u][2]));
            pb[1] = pk2(2.0f * bfhi(re[u][2]), -2.0f * bfhi(im[u][2]));
            pb[2] = pk2(2.0f * bflo(re[u][3]), -2.0f * bflo(im[u][3]));
            pb[3] = pk2(2.0f * bfhi(re[u][3]), -2.0f * bfhi(im[u][3]));
            size_t o = ((size_t)nt * KS_SSM + ks) * 64 + (lane & 15);
            ct[o + 16 * q0] = pa;
            ct[o + 16 * (q0 + 1)] = pb;
        }

    }
}

struct Frag9 { u32x4 a[4]; u32x4 b[5]; };

__device__ __forceinline__ void g2_load(Frag9& f, const char* __restrict__ ap, const char* __restrict__ bp,
                                        unsigned voff, int KS, int ks) {
#pragma unroll
    for (int i = 0; i < 4; ++i) f.a[i] = *(const u32x4*)(ap + (size_t)((i * KS + ks) * 1024) + voff);
#pragma unroll
    for (int j = 0; j < 5; ++j) f.b[j] = *(const u32x4*)(bp + (size_t)((j * KS + ks) * 1024) + voff);
    __builtin_amdgcn_sched_barrier(0);
}
__device__ __forceinline__ void g2_mma(const Frag9& f, f32x4 (&acc)[4][5]) {
#pragma unroll
    for (int i = 0; i < 4; ++i)
#pragma unroll
        for (int j = 0; j < 5; ++j)
            acc[i][j] = __builtin_amdgcn_mfma_f32_16x16x32_bf16(__builtin_bit_cast(bf16x8, f.b[j]),
                                                                __builtin_bit_cast(bf16x8, f.a[i]), acc[i][j], 0, 0, 0);
    __builtin_amdgcn_sched_barrier(0);
}

template <int KS, int KSTRIDE, int NSTEPS>
__device__ __forceinline__ void g2_run(const char* __restrict__ ap, const char* __restrict__ bp, unsigned voff,
                                       int k0, f32x4 (&acc)[4][5]) {
    Frag9 f0, f1, f2, f3;
    g2_load(f0, ap, bp, voff, KS, k0);
    g2_load(f1, ap, bp, voff, KS, k0 + KSTRIDE);
    g2_load(f2, ap, bp, voff, KS, k0 + 2 * KSTRIDE);
    g2_load(f3, ap, bp, voff, KS, k0 + 3 * KSTRIDE);
#pragma unroll
    for (int st = 0; st < NSTEPS; ++st) {
        const int kn = k0 + (st + 4) * KSTRIDE;
        if ((st & 3) == 0) { g2_mma(f0, acc); if (st + 4 < NSTEPS) g2_load(f0, ap, bp, voff, KS, kn); }
        if ((st & 3) == 1) { g2_mma(f1, acc); if (st + 4 < NSTEPS) g2_load(f1, ap, bp, voff, KS, kn); }
        if ((st & 3) == 2) { g2_mma(f2, acc); if (st + 4 < NSTEPS) g2_load(f2, ap, bp, voff, KS, kn); }
        if ((st & 3) == 3) { g2_mma(f3, acc); if (st + 4 < NSTEPS) g2_load(f3, ap, bp, voff, KS, kn); }
    }
}

struct K2Params {
    const unsigned short* x;
    const unsigned short* dskip;
    char* ws;
    float* out;
};

__global__ __launch_bounds__(512, 2) void k2_kernel(K2Params p) {
    __shared__ __attribute__((aligned(16))) float red[20 * 7 * 256];
    char* stamp_base = p.ws;
    STAMP(1, 0);
    const int bid = blockIdx.x;
    const int xcd = bid & 7, idx = bid >> 3;
    const int mb = (xcd >> 1) * 8 + (idx >> 2), nb = (xcd & 1) * 4 + (idx & 3);
    const int lane = threadIdx.x & 63;
    const int wid = __builtin_amdgcn_readfirstlane(threadIdx.x >> 6);
    const unsigned voff = lane * 16;
    f32x4 acc[4][5];
#pragma unroll
    for (int i = 0; i < 4; ++i)
#pragma unroll
        for (int j = 0; j < 5; ++j) acc[i][j] = (f32x4){0.f, 0.f, 0.f, 0.f};
    if (wid < 5) {
        const char* ap = p.ws + WS_HIDA + (size_t)(mb * 4) * KS_MLP * 1024;
        const char* bp = p.ws + WS_W2T + (size_t)(nb * 5) * KS_MLP * 1024;
        if (wid < 4) g2_run<KS_MLP, 5, 11>(ap, bp, voff, wid, acc);
        else g2_run<KS_MLP, 5, 10>(ap, bp, voff, wid, acc);
    } else {
        const char* ap = p.ws + WS_HA + (size_t)(mb * 4) * KS_SSM * 1024;
        const char* bp = p.ws + WS_CT + (size_t)(nb * 5) * KS_SSM * 1024;
        if (wid == 5) g2_run<KS_SSM, 3, 14>(ap, bp, voff, 0, acc);
        else g2_run<KS_SSM, 3, 13>(ap, bp, voff, wid - 5, acc);
    }
    STAMP(1, 2);
    const float* g = (const float*)(p.ws + WS_G);
    u32x2 ex[3], ed[3];
    float eg[3];
#pragma unroll
    for (int o = 0; o < 3; ++o) {
        int tau = wid + 8 * o;
        if (tau > 19) tau = 19;
        int i = tau / 5, j = tau % 5;
        int t = mb * 64 + i * 16 + (lane & 15);
        int d = nb * 80 + j * 16 + (lane >> 4) * 4;
        eg[o] = g[t];
        ex[o] = *(const u32x2*)(p.x + (size_t)t * DM + d);
        ed[o] = *(const u32x2*)(p.dskip + d);
    }
    f32x4* r4 = (f32x4*)red;
#pragma unroll
    for (int tau = 0; tau < 20; ++tau) {
        const int owner = tau % 8;
        if (wid != owner) {
            int wslot = wid < owner ? wid : wid - 1;
            r4[(tau * 7 + wslot) * 64 + lane] = acc[tau / 5][tau % 5];
        }
    }
    __syncthreads();
#pragma unroll
    for (int tau = 0; tau < 20; ++tau) {
        if (wid == tau % 8) {
            const int o = tau / 8, owner = tau % 8;
            f32x4 sm = (f32x4){0.f, 0.f, 0.f, 0.f}, ss = (f32x4){0.f, 0.f, 0.f, 0.f};
            if (owner < 5) sm = acc[tau / 5][tau % 5]; else ss = acc[tau / 5][tau % 5];
#pragma unroll
            for (int w = 0; w < 7; ++w) {
                const int writer = w < owner ? w : w + 1;
                f32x4 v = r4[(tau * 7 + w) * 64 + lane];
                if (writer < 5) sm += v; else ss += v;
            }
            const int i = tau / 5, j = tau % 5;
            int t = mb * 64 + i * 16 + (lane & 15);
            int d = nb * 80 + j * 16 + (lane >> 4) * 4;
            const float gg = eg[o];
            const float omg = bfr(1.0f - gg);
            float s0 = bfr(bflo(ex[o][0]) * bflo(ed[o][0]));
            float s1 = bfr(bfhi(ex[o][0]) * bfhi(ed[o][0]));
            float s2 = bfr(bflo(ex[o][1]) * bflo(ed[o][1]));
            float s3 = bfr(bfhi(ex[o][1]) * bfhi(ed[o][1]));
            f32x4 res;
            res[0] = gg * (ss[0] + s0) + bfr(omg * bfr(sm[0]));
            res[1] = gg * (ss[1] + s1) + bfr(omg * bfr(sm[1]));
            res[2] = gg * (ss[2] + s2) + bfr(omg * bfr(sm[2]));
            res[3] = gg * (ss[3] + s3) + bfr(omg * bfr(sm[3]));
            *(f32x4*)(p.out + (size_t)t * DM + d) = res;
        }
    }
    STAMP(1, 3);
    STAMP_DRAIN(1, 4);
}

extern "C" void kernel_launch(void* const* d_in, const int* in_sizes, int n_in, void* d_out, int out_size,
                              void* d_ws, size_t ws_size, hipStream_t stream) {
    const unsigned short* x = (const unsigned short*)d_in[0];
    const unsigned short* lre = (const unsigned short*)d_in[1];
    const unsigned short* lim = (const unsigned short*)d_in[2];
    const unsigned short* bre = (const unsigned short*)d_in[3];
    const unsigned short* bim = (const unsigned short*)d_in[4];
    const unsigned short* cre = (const unsigned short*)d_in[5];
    const unsigned short* cim = (const unsigned short*)d_in[6];
    const unsigned short* ldt = (const unsigned short*)d_in[7];
    const unsigned short* dskip = (const unsigned short*)d_in[8];
    const unsigned short* w1 = (const unsigned short*)d_in[10];
    const unsigned short* w2 = (const unsigned short*)d_in[11];
    const unsigned short* gw = (const unsigned short*)d_in[12];
    char* ws = (char*)d_ws;
    float* out = (float*)d_out;

    K1Params p1{x, lre, lim, bre, bim, cre, cim, ldt, w1, w2, gw, ws};
    k0_kernel<<<K0_GRID, 512, 0, stream>>>(p1);
    k1_kernel<<<K1_GRID, 512, 0, stream>>>(p1);
    K2Params p2{x, dskip, ws, out};
    k2_kernel<<<256, 512, 0, stream>>>(p2);
}
```

```cpp
#include <hip/hip_runtime.h>
#include <hip/hip_bf16.h>
#include <stdint.h>

typedef __attribute__((ext_vector_type(8))) __bf16 bf16x8;
typedef __attribute__((ext_vector_type(4))) __bf16 bf16x4;
typedef __attribute__((ext_vector_type(16))) float f32x16;
typedef __attribute__((ext_vector_type(4))) float f32x4;
typedef __attribute__((ext_vector_type(2))) float f32x2;
typedef __attribute__((ext_vector_type(4))) unsigned u32x4;
typedef __attribute__((ext_vector_type(2))) unsigned u32x2;
typedef __attribute__((address_space(3))) bf16x4* lds_bf16x4_ptr;

#define LSEQ 2048
#define DM 640
#define PN 640
#define FF 1728
#define CHUNK 64
#define NCHUNK (LSEQ / CHUNK)
#define KS_MLP (FF / 32)
#define KS_SSM (2 * PN / 32)
#define MT (LSEQ / 16)
#define NT (DM / 16)

#define WS_GRAN 0
#define WS_GRAN_SZ ((size_t)16 * PN * 2 * 8)
#define WS_HIDA (WS_GRAN + WS_GRAN_SZ)
#define WS_HIDA_SZ ((size_t)MT * KS_MLP * 1024)
#define WS_HA (WS_HIDA + WS_HIDA_SZ)
#define WS_HA_SZ ((size_t)MT * KS_SSM * 1024)
#define WS_W2T (WS_HA + WS_HA_SZ)
#define WS_W2T_SZ ((size_t)NT * KS_MLP * 1024)
#define WS_CT (WS_W2T + WS_W2T_SZ)
#define WS_CT_SZ ((size_t)NT * KS_SSM * 1024)
#define WS_G (WS_CT + WS_CT_SZ)

#define STAMP(kid, idx) do {} while (0)
#define STAMP_DRAIN(kid, idx) do {} while (0)
#define WSTAMP(kid, idx) do {} while (0)

__device__ __forceinline__ float bf2f(unsigned short u) { return __uint_as_float(((unsigned)u) << 16); }
__device__ __forceinline__ float bflo(unsigned u) { return __uint_as_float(u << 16); }
__device__ __forceinline__ float bfhi(unsigned u) { return __uint_as_float(u & 0xffff0000u); }
__device__ __forceinline__ float bfr(float x) { return (float)(__bf16)x; }
__device__ __forceinline__ unsigned short f2bf(float x) {
    __bf16 b = (__bf16)x;
    return __builtin_bit_cast(unsigned short, b);
}
#define WT_STORES 0
template <typename T>
__device__ __forceinline__ void st_out(T* ptr, T v) {
    static_assert(sizeof(T) == 16, "16-byte stores only");
    *ptr = v;
}
__device__ __forceinline__ unsigned pk2(float a, float b) { return f2bf(a) | ((unsigned)f2bf(b) << 16); }

__device__ __forceinline__ void sincos_acc(float x, float& s, float& c) {
    float kf = rintf(x * 0.63661977236758134f);
    float r = fmaf(kf, -1.5707962513e+00f, x);
    r = fmaf(kf, -7.5497894159e-08f, r);
    r = fmaf(kf, -5.3903029534e-15f, r);
    int k = (int)kf;
    float r2 = r * r;
    float sp = fmaf(r2, -1.9515295891e-04f, 8.3321608736e-03f);
    sp = fmaf(sp, r2, -1.6666654611e-01f);
    sp = fmaf(sp * r2, r, r);
    float cp = fmaf(r2, 2.443315711809948e-05f, -1.388731625493765e-03f);
    cp = fmaf(cp, r2, 4.166664568298827e-02f);
    cp = fmaf(cp * r2, r2, fmaf(-0.5f, r2, 1.0f));
    float ss = (k & 1) ? cp : sp;
    float cc = (k & 1) ? sp : cp;
    s = (k & 2) ? -ss : ss;
    c = ((k + 1) & 2) ? -cc : cc;
}

__device__ __forceinline__ void chan_lam(float lre, float lim, float ldt, float& Lr, float& Li) {
    float dt = bfr(expf(ldt));
    float a = dt * lre, b = dt * lim;
    float ea = expf(a);
    float sb, cb;
    sincos_acc(b, sb, cb);
    Lr = ea * cb;
    Li = ea * sb;
}
__device__ __forceinline__ void chan_consts(float lre, float lim, float ldt, float& Lr, float& Li,
                                            float& cr, float& ci) {
    float dt = bfr(expf(ldt));
    float a = dt * lre, b = dt * lim;
    float ea = expf(a);
    float sb, cb;
    sincos_acc(b, sb, cb);
    Lr = ea * cb;
    Li = ea * sb;
    float sh, chh;
    sincos_acc(0.5f * b, sh, chh);
    float nr = expm1f(a) * cb - 2.0f * sh * sh;
    float ni = Li;
    float den = lre * lre + lim * lim;
    float inv = 1.0f / den;
    cr = (nr * lre + ni * lim) * inv;
    ci = (ni * lre - nr * lim) * inv;
}

#define CMUL_R(ar, ai, br, bi) ((ar) * (br) - (ai) * (bi))
#define CMUL_I(ar, ai, br, bi) ((ar) * (bi) + (ai) * (br))

#define K1_SSM_BLOCKS 160
#define K1_MLP_BLOCKS 88
#define K1_GRID (K1_SSM_BLOCKS + K1_MLP_BLOCKS)
#define K0_GRID 20

__device__ __forceinline__ unsigned swz128(unsigned row, unsigned ch) {
    return row * 128u + ((ch ^ ((row >> 1) & 7u)) << 4);
}
__device__ __forceinline__ unsigned off_b(unsigned row, unsigned ch) {
    return 256u * row + 16u * (ch ^ (((row & 3u) << 2) | ((row >> 2) & 3u)));
}

struct K1Params {
    const unsigned short* x;
    const unsigned short* lre;
    const unsigned short* lim;
    const unsigned short* bre;
    const unsigned short* bim;
    const unsigned short* cre;
    const unsigned short* cim;
    const unsigned short* ldt;
    const unsigned short* w1;
    const unsigned short* w2;
    const unsigned short* gw;
    char* ws;
};

__device__ __forceinline__ bf16x4 tr_read_asm(unsigned lds_addr) {
    bf16x4 r;
    asm volatile("ds_read_b64_tr_b16 %0, %1" : "=v"(r) : "v"(lds_addr) : "memory");
    return r;
}
__device__ __forceinline__ bf16x8 lds_read128_asm(unsigned lds_addr) {
    bf16x8 r;
    asm volatile("ds_read_b128 %0, %1" : "=v"(r) : "v"(lds_addr) : "memory");
    return r;
}
template <int OFF>
__device__ __forceinline__ bf16x4 tr_read_off(unsigned lds_addr) {
    bf16x4 r;
    asm volatile("ds_read_b64_tr_b16 %0, %1 offset:%2" : "=v"(r) : "v"(lds_addr), "n"(OFF) : "memory");
    return r;
}
__device__ __forceinline__ unsigned opaque_s(unsigned v) {
    asm volatile("" : "+s"(v));
    return v;
}
__device__ __forceinline__ unsigned lds_addr_of(const char* ptr) {
    return (unsigned)(size_t)(__attribute__((address_space(3))) const char*)ptr;
}
#define K1_LDS_BYTES (3 * 53248 + 768)
#define RAW_BARRIER() do { asm volatile("s_waitcnt lgkmcnt(0)" ::: "memory"); __builtin_amdgcn_s_barrier(); } while (0)
#define GLDS16(SRC, DST) __builtin_amdgcn_global_load_lds((const unsigned*)(SRC), (unsigned*)(DST), 16, 0, 0)
#define SCHED_FENCE() __builtin_amdgcn_sched_barrier(0)

__device__ __forceinline__ void prep_slot(const K1Params& p, int slot, char* img, int lane) {
    const bool do_g = slot < 512;
    const int l16 = lane & 15;
    const int tg = (do_g ? slot : 0) * 4 + (lane >> 4);
    u32x4 xv[5], gv[5];
    if (do_g) {
#pragma unroll
        for (int i = 0; i < 5; ++i) {
            gv[i] = *(const u32x4*)(p.gw + (l16 + 16 * i) * 8);
            xv[i] = *(const u32x4*)(p.x + (size_t)tg * DM + (l16 + 16 * i) * 8);
        }
    }
    if (slot < KS_MLP * 10) {
        u32x4* w2t = (u32x4*)(p.ws + WS_W2T);
        const int ks = slot / 10, ng = slot % 10;
        u32x4 v[4];
#pragma unroll
        for (int i = 0; i < 4; ++i) {
            int idx = lane + 64 * i, row = idx >> 3, c8 = idx & 7;
            v[i] = *(const u32x4*)(p.w2 + (size_t)(32 * ks + row) * DM + ng * 64 + c8 * 8);
        }
#pragma unroll
        for (int i = 0; i < 4; ++i) {
            int idx = lane + 64 * i, row = idx >> 3, c8 = idx & 7;
            *(u32x4*)(img + row * 128 + c8 * 16) = v[i];
        }
        __builtin_amdgcn_fence(__ATOMIC_RELEASE, "wavefront");
        __builtin_amdgcn_wave_barrier();
        __builtin_amdgcn_fence(__ATOMIC_ACQUIRE, "wavefront");
        const int q = lane >> 4, qq = (lane & 15) >> 2, pp = lane & 3;
#pragma unroll
        for (int c = 0; c < 4; ++c) {
            int r0 = 16 * (q >> 1) + 4 * (q & 1);
            unsigned a0 = (r0 + qq) * 128 + c * 32 + pp * 8;
            unsigned a1 = a0 + 8 * 128;
            bf16x4 lo = __builtin_amdgcn_ds_read_tr16_b64_v4bf16((lds_bf16x4_ptr)(img + a0));
            bf16x4 hi = __builtin_amdgcn_ds_read_tr16_b64_v4bf16((lds_bf16x4_ptr)(img + a1));
            bf16x8 f = __builtin_shufflevector(lo, hi, 0, 1, 2, 3, 4, 5, 6, 7);
            w2t[((size_t)(ng * 4 + c) * KS_MLP + ks) * 64 + lane] = __builtin_bit_cast(u32x4, f);
        }
    } else if (slot < KS_MLP * 10 + 400) {
        u32x4* ct = (u32x4*)(p.ws + WS_CT);
        u32x4 re[2], im[2];
#pragma unroll
        for (int u = 0; u < 2; ++u) {
            int task = (slot - KS_MLP * 10) * 2 + u;
            int nt = task / 20, pg = task % 20;
            int d = nt * 16 + (lane & 15), pc = pg * 32 + (lane >> 4) * 8;
            re[u] = *(const u32x4*)(p.cre + (size_t)d * PN + pc);
            im[u] = *(const u32x4*)(p.cim + (size_t)d * PN + pc);
        }
#pragma unroll
        for (int u = 0; u < 2; ++u) {
            int task = (slot - KS_MLP * 10) * 2 + u;
            int nt = task / 20, pg = task % 20;
            int c = lane >> 4;
            int ks = pg * 2 + (c >> 1), q0 = 2 * (c & 1);
            u32x4 pa, pb;
            pa[0] = pk2(2.0f * bflo(re[u][0]), -2.0f * bflo(im[u][0]));
            pa[1] = pk2(2.0f * bfhi(re[u][0]), -2.0f * bfhi(im[u][0]));
            pa[2] = pk2(2.0f * bflo(re[u][1]), -2.0f * bflo(im[u][1]));
            pa[3] = pk2(2.0f * bfhi(re[u][1]), -2.0f * bfhi(im[u][1]));
            pb[0] = pk2(2.0f * bflo(re[u][2]), -2.0f * bflo(im[u][2]));
            pb[1] = pk2(2.0f * bfhi(re[u][2]), -2.0f * bfhi(im[u][2]));
            pb[2] = pk2(2.0f * bflo(re[u][3]), -2.0f * bflo(im[u][3]));
            pb[3] = pk2(2.0f * bfhi(re[u][3]), -2.0f * bfhi(im[u][3]));
            size_t o = ((size_t)nt * KS_SSM + ks) * 64 + (lane & 15);
            ct[o + 16 * q0] = pa;
            ct[o + 16 * (q0 + 1)] = pb;
        }
    }
    if (do_g) {
        float sum = 0.f;
#pragma unroll
        for (int i = 0; i < 5; ++i)
#pragma unroll
            for (int e = 0; e < 4; ++e) {
                sum += bfr(bflo(xv[i][e]) * bflo(gv[i][e]));
                sum += bfr(bfhi(xv[i][e]) * bfhi(gv[i][e]));
            }
#pragma unroll
        for (int o = 8; o > 0; o >>= 1) sum += __shfl_xor(sum, o);
        if (l16 == 0) {
            float z = bfr(sum);
            ((float*)(p.ws + WS_G))[tg] = bfr(1.0f / (1.0f + expf(-z)));
        }
    }
}

#define SS_STAGE 32768
#define SS_CONST_OFF (4 * SS_STAGE)
__device__ __forceinline__ void k1_ssm_tile(const K1Params& p, char* smem, int mtile, int ntile, int sbid) {
    const int tid = threadIdx.x;
    const int lane = tid & 63;
    const int wid = __builtin_amdgcn_readfirstlane(tid >> 6);
    const int t0 = mtile * 128;
    const int wt = wid >> 1, wn = wid & 1;
    char* stamp_base = p.ws;
    STAMP(0, 0);

    const unsigned short* xs_src[2];
    const unsigned short* ws_src[2];
#pragma unroll
    for (int i = 0; i < 2; ++i) {
        unsigned row = (2 * wid + i) * 8 + (lane >> 3);
        unsigned chk = (lane & 7) ^ ((row >> 1) & 7);
        xs_src[i] = p.x + (size_t)(t0 + row) * DM + chk * 8;
        unsigned grp = row >> 5;
        const unsigned short* base = (grp & 1) ? p.bim : p.bre;
        ws_src[i] = base + (size_t)(ntile * 64 + (row & 31) + 32 * (grp >> 1)) * DM + chk * 8;
    }
#define SS_GLDS(KT)                                                                    \
    do {                                                                               \
        char* st_ = smem + ((KT) & 3) * SS_STAGE;                                      \
        GLDS16(xs_src[0] + (KT) * 64, st_ + (2 * wid) * 1024);                         \
        GLDS16(xs_src[1] + (KT) * 64, st_ + (2 * wid + 1) * 1024);                     \
        GLDS16(ws_src[0] + (KT) * 64, st_ + 16384 + (2 * wid) * 1024);                 \
        GLDS16(ws_src[1] + (KT) * 64, st_ + 16384 + (2 * wid + 1) * 1024);             \
    } while (0)
    {
        const int cc = ntile * 64 + 2 * (lane & 31);
        __builtin_amdgcn_global_load_lds((const unsigned*)(p.lre + cc), (unsigned*)(smem + SS_CONST_OFF), 4, 0, 0);
        __builtin_amdgcn_global_load_lds((const unsigned*)(p.lim + cc), (unsigned*)(smem + SS_CONST_OFF + 256), 4, 0, 0);
        __builtin_amdgcn_global_load_lds((const unsigned*)(p.ldt + cc), (unsigned*)(smem + SS_CONST_OFF + 512), 4, 0, 0);
    }
    SS_GLDS(0);
    SS_GLDS(1);
    SS_GLDS(2);
    SS_GLDS(3);

    f32x16 accR, accI;
#pragma unroll
    for (int r = 0; r < 16; ++r) { accR[r] = 0.f; accI[r] = 0.f; }
    const unsigned l31 = lane & 31, lh = lane >> 5;
    const int ch = ntile * 64 + wn * 32 + l31;
    const unsigned xa0 = wt * 32 + l31;
    const unsigned ba0 = wn * 64 + l31, ba1 = ba0 + 32;
    const unsigned sbase = lds_addr_of(smem);

    unsigned sxo[4], sbo0[4], sbo1[4];
#pragma unroll
    for (int q = 0; q < 4; ++q) {
        sxo[q] = swz128(xa0, 2 * q + lh);
        sbo0[q] = 16384 + swz128(ba0, 2 * q + lh);
        sbo1[q] = 16384 + swz128(ba1, 2 * q + lh);
    }
#define SS_FRAGS(F, KT, S)                                                             \
    do {                                                                               \
        const unsigned xb_ = opaque_s(sbase + ((KT) & 3) * SS_STAGE);                  \
        F[0] = lds_read128_asm(xb_ + sxo[S]);                                          \
        F[1] = lds_read128_asm(xb_ + sbo0[S]);                                         \
        F[2] = lds_read128_asm(xb_ + sbo1[S]);                                         \
    } while (0)
#define SS_WAIT(F, N) asm volatile("s_waitcnt lgkmcnt(" #N ")" : "+v"(F[0]), "+v"(F[1]), "+v"(F[2])::"memory")
#define SS_MMA(F)                                                                      \
    do {                                                                               \
        accR = __builtin_amdgcn_mfma_f32_32x32x16_bf16(F[0], F[1], accR, 0, 0, 0);     \
        accI = __builtin_amdgcn_mfma_f32_32x32x16_bf16(F[0], F[2], accI, 0, 0, 0);     \
    } while (0)
    bf16x8 fA[3], fB[3];
#define SS_STEP(KT)                                                                    \
    do {                                                                               \
        SS_WAIT(fA, 3); SS_MMA(fA); SCHED_FENCE();                                     \
        SS_FRAGS(fA, KT, 2); SCHED_FENCE();                                            \
        SS_WAIT(fB, 3); SS_MMA(fB); SCHED_FENCE();                                     \
        SS_FRAGS(fB, KT, 3); SCHED_FENCE();                                            \
        SS_WAIT(fA, 3); SS_MMA(fA); SCHED_FENCE();                                     \
        if ((KT) + 1 < 10) {                                                           \
            if ((KT) + 3 < 10) asm volatile("s_waitcnt vmcnt(8)" ::: "memory");        \
            else if ((KT) + 2 < 10) asm volatile("s_waitcnt vmcnt(4)" ::: "memory");   \
            else asm volatile("s_waitcnt vmcnt(0)" ::: "memory");                      \
            RAW_BARRIER();                                                             \
            if ((KT) + 4 < 10) SS_GLDS((KT) + 4);                                      \
            SS_FRAGS(fA, (KT) + 1, 0); SCHED_FENCE();                                  \
            SS_WAIT(fB, 3);                                                            \
        } else {                                                                       \
            SS_WAIT(fB, 0);                                                            \
        }                                                                              \
        SS_MMA(fB); SCHED_FENCE();                                                     \
        if ((KT) + 1 < 10) SS_FRAGS(fB, (KT) + 1, 1);                                  \
        SCHED_FENCE();                                                                 \
    } while (0)
    asm volatile("s_waitcnt vmcnt(12)" ::: "memory");
    RAW_BARRIER();
    SS_FRAGS(fA, 0, 0);
    SS_FRAGS(fB, 0, 1);
    SCHED_FENCE();
    float Lr, Li, cr, ci;
    {
        const unsigned short* cst = (const unsigned short*)(smem + SS_CONST_OFF);
        const int cl0 = wn * 32 + l31;
        chan_consts(bf2f(cst[cl0]), bf2f(cst[128 + cl0]), bf2f(cst[256 + cl0]), Lr, Li, cr, ci);
    }
    const float L2r = CMUL_R(Lr, Li, Lr, Li), L2i = CMUL_I(Lr, Li, Lr, Li);
    const float L3r = CMUL_R(L2r, L2i, Lr, Li), L3i = CMUL_I(L2r, L2i, Lr, Li);
    const float L4r = CMUL_R(L2r, L2i, L2r, L2i), L4i = CMUL_I(L2r, L2i, L2r, L2i);
    float Pr = L4r, Pi = L4i;
#pragma unroll
    for (int i = 0; i < 3; ++i) { float nr = Pr * Pr - Pi * Pi, ni = 2.f * Pr * Pi; Pr = nr; Pi = ni; }
    SCHED_FENCE();
    SS_STEP(0); SS_STEP(1); SS_STEP(2); SS_STEP(3); SS_STEP(4);
    SS_STEP(5); SS_STEP(6); SS_STEP(7); SS_STEP(8); SS_STEP(9);
#undef SS_STEP
#undef SS_MMA
#undef SS_WAIT
#undef SS_FRAGS
#undef SS_GLDS
    STAMP(0, 1);

    f32x16 vr, vi;
#pragma unroll
    for (int r = 0; r < 16; ++r) {
        vr[r] = cr * accR[r] - ci * accI[r];
        vi[r] = cr * accI[r] + ci * accR[r];
    }
#pragma unroll
    for (int j = 0; j < 4; ++j)
#pragma unroll
        for (int i = 1; i < 4; ++i) {
            int b = 4 * j + i;
            float pr = vr[b - 1], pi = vi[b - 1];
            vr[b] += Lr * pr - Li * pi;
            vi[b] += Lr * pi + Li * pr;
        }
    float E0r[4], E0i[4], E1r[4], E1i[4];
#pragma unroll
    for (int g = 0; g < 4; ++g) {
        unsigned er = __float_as_uint(vr[4 * g + 3]), ei = __float_as_uint(vi[4 * g + 3]);
        auto sr = __builtin_amdgcn_permlane32_swap(er, er, false, false);
        auto si = __builtin_amdgcn_permlane32_swap(ei, ei, false, false);
        E0r[g] = __uint_as_float(sr[0]); E0i[g] = __uint_as_float(si[0]);
        E1r[g] = __uint_as_float(sr[1]); E1i[g] = __uint_as_float(si[1]);
    }
    float aR = 0.f, aI = 0.f;
#pragma unroll
    for (int g = 0; g < 4; ++g) {
        float c1r = L4r * aR - L4i * aI + E0r[g], c1i = L4r * aI + L4i * aR + E0i[g];
        aR = L4r * c1r - L4i * c1i + E1r[g];
        aI = L4r * c1i + L4i * c1r + E1i[g];
    }
    f32x2* aggL = (f32x2*)smem;
    f32x2* hinL = (f32x2*)(smem + 2048);
    const int cl = wn * 32 + l31;
    STAMP(0, 24);
    __syncthreads();
    if (lh == 0) aggL[wt * 64 + cl] = (f32x2){aR, aI};
    __syncthreads();
    STAMP(0, 25);
    float bR = 0.f, bI = 0.f, inR = 0.f, inI = 0.f;
#pragma unroll
    for (int w = 0; w < 4; ++w) {
        if (w == wt) { inR = bR; inI = bI; }
        f32x2 a = aggL[w * 64 + cl];
        float nr = Pr * bR - Pi * bI + a[0], ni = Pr * bI + Pi * bR + a[1];
        bR = nr; bI = ni;
    }
    if (wt == 0) {
        unsigned long long* gran = (unsigned long long*)(p.ws + WS_GRAN);
        const float myv = lh ? bI : bR;
        __hip_atomic_store(&gran[((size_t)mtile * PN + ch) * 2 + lh],
                           (1ull << 32) | (unsigned long long)__float_as_uint(myv), __ATOMIC_RELAXED,
                           __HIP_MEMORY_SCOPE_AGENT);
        float Qr = Pr * Pr - Pi * Pi, Qi = 2.f * Pr * Pi;
        { float nr = Qr * Qr - Qi * Qi, ni = 2.f * Qr * Qi; Qr = nr; Qi = ni; }
        unsigned val[15];
        for (unsigned spin = 0; spin < (1u << 24); ++spin) {
            bool ok = true;
#pragma unroll
            for (int m = 0; m < 15; ++m) {
                if (m < mtile) {
                    unsigned long long x = __hip_atomic_load(&gran[((size_t)m * PN + ch) * 2 + lh], __ATOMIC_RELAXED,
                                                             __HIP_MEMORY_SCOPE_AGENT);
                    val[m] = (unsigned)x;
                    ok = ok && ((x >> 32) == 1ull);
                }
            }
            if (__all(ok)) break;
            __builtin_amdgcn_s_sleep(1);
        }
        STAMP(0, 26);
        float hR = 0.f, hI = 0.f;
#pragma unroll
        for (int m = 0; m < 15; ++m) {
            if (m < mtile) {
                auto sw = __builtin_amdgcn_permlane32_swap(val[m], val[m], false, false);
                float er = __uint_as_float(sw[0]), ei = __uint_as_float(sw[1]);
                float nr = Qr * hR - Qi * hI + er, ni = Qr * hI + Qi * hR + ei;
                hR = nr; hI = ni;
            }
        }
        if (lh == 0) hinL[cl] = (f32x2){hR, hI};
    } else {
        prep_slot(p, sbid * 6 + (wid - 2), smem + 49152 + wid * 4096, lane);
    }
    __syncthreads();
    STAMP(0, 27);
    float cR, cI;
    {
        f32x2 hb = hinL[cl];
        float hR = hb[0], hI = hb[1];
#pragma unroll
        for (int w = 0; w < 3; ++w)
            if (w < wt) { float nr = Pr * hR - Pi * hI, ni = Pr * hI + Pi * hR; hR = nr; hI = ni; }
        cR = hR + inR; cI = hI + inI;
    }
#pragma unroll
    for (int j = 0; j < 4; ++j) {
        float c0r = cR, c0i = cI;
        float c1r = L4r * cR - L4i * cI + E0r[j];
        float c1i = L4r * cI + L4i * cR + E0i[j];
        cR = L4r * c1r - L4i * c1i + E1r[j];
        cI = L4r * c1i + L4i * c1r + E1i[j];
        float inr = lh ? c1r : c0r, ini = lh ? c1i : c0i;
        vr[4 * j + 0] += Lr * inr - Li * ini;
        vi[4 * j + 0] += Lr * ini + Li * inr;
        vr[4 * j + 1] += L2r * inr - L2i * ini;
        vi[4 * j + 1] += L2r * ini + L2i * inr;
        vr[4 * j + 2] += L3r * inr - L3i * ini;
        vi[4 * j + 2] += L3r * ini + L3i * inr;
        vr[4 * j + 3] += L4r * inr - L4i * ini;
        vi[4 * j + 3] += L4r * ini + L4i * inr;
    }
    STAMP(0, 3);
    char* tile = smem + 4096 + wid * 4608;
#pragma unroll
    for (int r = 0; r < 16; ++r) {
        int tl = (r & 3) + 8 * (r >> 2) + 4 * lh;
        *(unsigned*)(tile + tl * 144 + l31 * 4) = pk2(vr[r], vi[r]);
    }
    __builtin_amdgcn_fence(__ATOMIC_RELEASE, "wavefront");
    __builtin_amdgcn_wave_barrier();
    __builtin_amdgcn_fence(__ATOMIC_ACQUIRE, "wavefront");
    u32x4* ha = (u32x4*)(p.ws + WS_HA);
#pragma unroll
    for (int mq = 0; mq < 2; ++mq)
#pragma unroll
        for (int k2 = 0; k2 < 2; ++k2) {
            u32x4 v = *(const u32x4*)(tile + (mq * 16 + (lane & 15)) * 144 + (k2 * 4 + (lane >> 4)) * 16);
            int mt = ((t0 + wt * 32) >> 4) + mq;
            int ks = ((ntile * 64 + wn * 32) >> 4) + k2;
            ha[((size_t)mt * KS_SSM + ks) * 64 + lane] = v;
        }
    STAMP_DRAIN(0, 4);
}

#define ML_STAGE 53248
__device__ __forceinline__ void k1_mlp_tile(const K1Params& p, char* smem, int mtile, int ntile) {
    const int tid = threadIdx.x;
    const int lane = tid & 63;
    const int wid = __builtin_amdgcn_readfirstlane(tid >> 6);
    const int t0 = mtile * 256;
    char* stamp_base = p.ws;
    STAMP(0, 0);

    const unsigned short* xs_src[4];
#pragma unroll
    for (int i = 0; i < 4; ++i) {
        unsigned row = (4 * wid + i) * 8 + (lane >> 3);
        unsigned chk = (lane & 7) ^ ((row >> 1) & 7);
        xs_src[i] = p.x + (size_t)(t0 + row) * DM + chk * 8;
    }
    const unsigned short* wa_src[2];
#pragma unroll
    for (int i = 0; i < 2; ++i) {
        unsigned row = (2 * wid + i) * 4 + (lane >> 4);
        unsigned chk = (lane & 15) ^ (((row & 3u) << 2) | ((row >> 2) & 3u));
        wa_src[i] = p.w1 + (size_t)row * FF + ntile * 160 + chk * 8;
    }
    const unsigned short* wb_src;
    {
        unsigned row = (wid & 3) * 16 + (lane >> 2);
        int n = ntile * 160 + 128 + (lane & 3) * 8;
        if (n > FF - 8) n = FF - 8;
        wb_src = p.w1 + (size_t)row * FF + n;
    }
#define ML_GLDS(KT)                                                                          \
    do {                                                                                     \
        char* st_ = smem + ((KT) % 3) * ML_STAGE;                                            \
        _Pragma("unroll") for (int i = 0; i < 4; ++i) GLDS16(xs_src[i] + (KT) * 64, st_ + (4 * wid + i) * 1024); \
        _Pragma("unroll") for (int i = 0; i < 2; ++i) GLDS16(wa_src[i] + (size_t)(KT) * 64 * FF, st_ + 32768 + (2 * wid + i) * 1024); \
        GLDS16(wb_src + (size_t)(KT) * 64 * FF, st_ + 49152 + (wid & 3) * 1024);             \
    } while (0)
    ML_GLDS(0);
    ML_GLDS(1);
    ML_GLDS(2);

    f32x16 acc[5];
#pragma unroll
    for (int i = 0; i < 5; ++i)
#pragma unroll
        for (int r = 0; r < 16; ++r) acc[i][r] = 0.f;
    const unsigned l31 = lane & 31, lh = lane >> 5;
    const unsigned xa0 = wid * 32 + l31;
    const unsigned tq = (lane & 15) >> 2, tp = lane & 3, tblk = (lane >> 4) & 1;
    const unsigned sbase = lds_addr_of(smem);

    unsigned mxo[4], mwo[8], mbo;
#pragma unroll
    for (int q = 0; q < 4; ++q) mxo[q] = swz128(xa0, 2 * q + lh);
#pragma unroll
    for (int c = 0; c < 4; ++c) {
        mwo[2 * c] = 32768 + off_b(8 * lh + 0 + tq, 4 * c + 2 * tblk + (tp >> 1)) + 8 * (tp & 1);
        mwo[2 * c + 1] = 32768 + off_b(8 * lh + 4 + tq, 4 * c + 2 * tblk + (tp >> 1)) + 8 * (tp & 1);
    }
    mbo = 49152 + (8 * lh + tq) * 64 + tblk * 32 + tp * 8;
#define ML_FRAGS(X, T, KT, S)                                                                \
    do {                                                                                     \
        const unsigned xb_ = opaque_s(sbase + ((KT) % 3) * ML_STAGE);                        \
        X = lds_read128_asm(xb_ + mxo[S]);                                                   \
        _Pragma("unroll") for (int c = 0; c < 8; ++c) T[c] = tr_read_off<4096 * (S)>(xb_ + mwo[c]); \
        T[8] = tr_read_off<1024 * (S)>(xb_ + mbo);                                           \
        T[9] = tr_read_off<1024 * (S) + 256>(xb_ + mbo);                                     \
    } while (0)
#define ML_WAIT(X, T, N)                                                                     \
    asm volatile("s_waitcnt lgkmcnt(" #N ")" : "+v"(X), "+v"(T[0]), "+v"(T[1]), "+v"(T[2]), "+v"(T[3]), "+v"(T[4]), \
                 "+v"(T[5]), "+v"(T[6]), "+v"(T[7]), "+v"(T[8]), "+v"(T[9])::"memory")
#define ML_MMA(X, T)                                                                         \
    do {                                                                                     \
        _Pragma("unroll") for (int c = 0; c < 5; ++c) {                                      \
            bf16x8 wf_ = __builtin_shufflevector(T[2 * c], T[2 * c + 1], 0, 1, 2, 3, 4, 5, 6, 7); \
            acc[c] = __builtin_amdgcn_mfma_f32_32x32x16_bf16(wf_, X, acc[c], 0, 0, 0);       \
        }                                                                                    \
    } while (0)
    bf16x8 xA, xB;
    bf16x4 tA[10], tB[10];
#define ML_STEP(KT)                                                                          \
    do {                                                                                     \
        ML_WAIT(xA, tA, 11); ML_MMA(xA, tA); SCHED_FENCE();                                  \
        ML_FRAGS(xA, tA, KT, 2); SCHED_FENCE();                                              \
        ML_WAIT(xB, tB, 11); ML_MMA(xB, tB); SCHED_FENCE();                                  \
        ML_FRAGS(xB, tB, KT, 3); SCHED_FENCE();                                              \
        ML_WAIT(xA, tA, 11); ML_MMA(xA, tA); SCHED_FENCE();                                  \
        if ((KT) + 1 < 10) {                                                                 \
            if ((KT) + 2 < 10) asm volatile("s_waitcnt vmcnt(7)" ::: "memory");              \
            else asm volatile("s_waitcnt vmcnt(0)" ::: "memory");                            \
            RAW_BARRIER();                                                                   \
            if ((KT) + 3 < 10) ML_GLDS((KT) + 3);                                            \
            ML_FRAGS(xA, tA, (KT) + 1, 0); SCHED_FENCE();                                    \
            ML_WAIT(xB, tB, 11);                                                             \
        } else {                                                                             \
            ML_WAIT(xB, tB, 0);                                                              \
        }                                                                                    \
        ML_MMA(xB, tB); SCHED_FENCE();                                                       \
        if ((KT) + 1 < 10) ML_FRAGS(xB, tB, (KT) + 1, 1);                                    \
        SCHED_FENCE();                                                                       \
    } while (0)
    asm volatile("s_waitcnt vmcnt(14)" ::: "memory");
    RAW_BARRIER();
    ML_FRAGS(xA, tA, 0, 0);
    ML_FRAGS(xB, tB, 0, 1);
    SCHED_FENCE();
    ML_STEP(0); ML_STEP(1); ML_STEP(2); ML_STEP(3); ML_STEP(4);
    ML_STEP(5); ML_STEP(6); ML_STEP(7); ML_STEP(8); ML_STEP(9);
#undef ML_STEP
#undef ML_MMA
#undef ML_WAIT
#undef ML_FRAGS
#undef ML_GLDS
    STAMP(0, 2);
    u32x4* hid = (u32x4*)(p.ws + WS_HIDA);
    const int t = t0 + wid * 32 + l31;
    const int mt = t >> 4;
#pragma unroll
    for (int fi = 0; fi < 5; ++fi) {
        int f0 = ntile * 160 + fi * 32;
        if (f0 < FF) {
            int ks = f0 >> 5;
#pragma unroll
            for (int half = 0; half < 2; ++half) {
                u32x4 pk;
#pragma unroll
                for (int j = 0; j < 4; ++j) {
                    float v0 = acc[fi][8 * half + 2 * j];
                    float v1 = acc[fi][8 * half + 2 * j + 1];
                    v0 = v0 > 0.f ? v0 : 0.f;
                    v1 = v1 > 0.f ? v1 : 0.f;
                    pk[j] = pk2(v0, v1);
                }
                int q = 2 * half + lh;
                hid[((size_t)mt * KS_MLP + ks) * 64 + (t & 15) + 16 * q] = pk;
            }
        }
    }
    STAMP_DRAIN(0, 5);
}

__global__ __launch_bounds__(512, 2) void k1_kernel(K1Params p) {
    __shared__ __attribute__((aligned(16))) char smem[K1_LDS_BYTES];
    int bid = blockIdx.x;
    if (bid < K1_SSM_BLOCKS) {
        int xcd = bid & 7, idx = bid >> 3;
        k1_ssm_tile(p, smem, (xcd >> 1) * 4 + (idx & 3), (xcd & 1) * 5 + (idx >> 2), bid);
    } else {
        bid -= K1_SSM_BLOCKS;
        k1_mlp_tile(p, smem, bid & 7, bid >> 3);
    }
}

__global__ __launch_bounds__(512) void k0_kernel(u32x4* g) {
    g[blockIdx.x * 512 + threadIdx.x] = (u32x4){0u, 0u, 0u, 0u};
}

struct Frag9 { u32x4 a[4]; u32x4 b[5]; };

__device__ __forceinline__ void g2_load(Frag9& f, const char* __restrict__ ap, const char* __restrict__ bp,
                                        unsigned voff, int KS, int ks) {
#pragma unroll
    for (int i = 0; i < 4; ++i) f.a[i] = *(const u32x4*)(ap + (size_t)((i * KS + ks) * 1024) + voff);
#pragma unroll
    for (int j = 0; j < 5; ++j) f.b[j] = *(const u32x4*)(bp + (size_t)((j * KS + ks) * 1024) + voff);
    __builtin_amdgcn_sched_barrier(0);
}
__device__ __forceinline__ void g2_mma(const Frag9& f, f32x4 (&acc)[4][5]) {
#pragma unroll
    for (int i = 0; i < 4; ++i)
#pragma unroll
        for (int j = 0; j < 5; ++j)
            acc[i][j] = __builtin_amdgcn_mfma_f32_16x16x32_bf16(__builtin_bit_cast(bf16x8, f.b[j]),
                                                                __builtin_bit_cast(bf16x8, f.a[i]), acc[i][j], 0, 0, 0);
    __builtin_amdgcn_sched_barrier(0);
}

template <int KS, int KSTRIDE, int NSTEPS>
__device__ __forceinline__ void g2_run(const char* __restrict__ ap, const char* __restrict__ bp, unsigned voff,
                                       int k0, f32x4 (&acc)[4][5]) {
    Frag9 f0, f1, f2, f3;
    g2_load(f0, ap, bp, voff, KS, k0);
    g2_load(f1, ap, bp, voff, KS, k0 + KSTRIDE);
    g2_load(f2, ap, bp, voff, KS, k0 + 2 * KSTRIDE);
    g2_load(f3, ap, bp, voff, KS, k0 + 3 * KSTRIDE);
#pragma unroll
    for (int st = 0; st < NSTEPS; ++st) {
        const int kn = k0 + (st + 4) * KSTRIDE;
        if ((st & 3) == 0) { g2_mma(f0, acc); if (st + 4 < NSTEPS) g2_load(f0, ap, bp, voff, KS, kn); }
        if ((st & 3) == 1) { g2_mma(f1, acc); if (st + 4 < NSTEPS) g2_load(f1, ap, bp, voff, KS, kn); }
        if ((st & 3) == 2) { g2_mma(f2, acc); if (st + 4 < NSTEPS) g2_load(f2, ap, bp, voff, KS, kn); }
        if ((st & 3) == 3) { g2_mma(f3, acc); if (st + 4 < NSTEPS) g2_load(f3, ap, bp, voff, KS, kn); }
    }
}

struct K2Params {
    const unsigned short* x;
    const unsigned short* dskip;
    char* ws;
    float* out;
};

__global__ __launch_bounds__(512, 2) void k2_kernel(K2Params p) {
    __shared__ __attribute__((aligned(16))) float red[20 * 7 * 256];
    char* stamp_base = p.ws;
    STAMP(1, 0);
    const int bid = blockIdx.x;
    const int xcd = bid & 7, idx = bid >> 3;
    const int mb = (xcd >> 1) * 8 + (idx >> 2), nb = (xcd & 1) * 4 + (idx & 3);
    const int lane = threadIdx.x & 63;
    const int wid = __builtin_amdgcn_readfirstlane(threadIdx.x >> 6);
    const unsigned voff = lane * 16;
    f32x4 acc[4][5];
#pragma unroll
    for (int i = 0; i < 4; ++i)
#pragma unroll
        for (int j = 0; j < 5; ++j) acc[i][j] = (f32x4){0.f, 0.f, 0.f, 0.f};
    if (wid < 5) {
        const char* ap = p.ws + WS_HIDA + (size_t)(mb * 4) * KS_MLP * 1024;
        const char* bp = p.ws + WS_W2T + (size_t)(nb * 5) * KS_MLP * 1024;
        if (wid < 4) g2_run<KS_MLP, 5, 11>(ap, bp, voff, wid, acc);
        else g2_run<KS_MLP, 5, 10>(ap, bp, voff, wid, acc);
    } else {
        const char* ap = p.ws + WS_HA + (size_t)(mb * 4) * KS_SSM * 1024;
        const char* bp = p.ws + WS_CT + (size_t)(nb * 5) * KS_SSM * 1024;
        if (wid == 5) g2_run<KS_SSM, 3, 14>(ap, bp, voff, 0, acc);
        else g2_run<KS_SSM, 3, 13>(ap, bp, voff, wid - 5, acc);
    }
    STAMP(1, 2);
    const float* g = (const float*)(p.ws + WS_G);
    u32x2 ex[3], ed[3];
    float eg[3];
#pragma unroll
    for (int o = 0; o < 3; ++o) {
        int tau = wid + 8 * o;
        if (tau > 19) tau = 19;
        int i = tau / 5, j = tau % 5;
        int t = mb * 64 + i * 16 + (lane & 15);
        int d = nb * 80 + j * 16 + (lane >> 4) * 4;
        eg[o] = g[t];
        ex[o] = *(const u32x2*)(p.x + (size_t)t * DM + d);
        ed[o] = *(const u32x2*)(p.dskip + d);
    }
    f32x4* r4 = (f32x4*)red;
#pragma unroll
    for (int tau = 0; tau < 20; ++tau) {
        const int owner = tau % 8;
        if (wid != owner) {
            int wslot = wid < owner ? wid : wid - 1;
            r4[(tau * 7 + wslot) * 64 + lane] = acc[tau / 5][tau % 5];
        }
    }
    __syncthreads();
#pragma unroll
    for (int tau = 0; tau < 20; ++tau) {
        if (wid == tau % 8) {
            const int o = tau / 8, owner = tau % 8;
            f32x4 sm = (f32x4){0.f, 0.f, 0.f, 0.f}, ss = (f32x4){0.f, 0.f, 0.f, 0.f};
            if (owner < 5) sm = acc[tau / 5][tau % 5]; else ss = acc[tau / 5][tau % 5];
#pragma unroll
            for (int w = 0; w < 7; ++w) {
                const int writer = w < owner ? w : w + 1;
                f32x4 v = r4[(tau * 7 + w) * 64 + lane];
                if (writer < 5) sm += v; else ss += v;
            }
            const int i = tau / 5, j = tau % 5;
            int t = mb * 64 + i * 16 + (lane & 15);
            int d = nb * 80 + j * 16 + (lane >> 4) * 4;
            const float gg = eg[o];
            const float omg = bfr(1.0f - gg);
            float s0 = bfr(bflo(ex[o][0]) * bflo(ed[o][0]));
            float s1 = bfr(bfhi(ex[o][0]) * bfhi(ed[o][0]));
            float s2 = bfr(bflo(ex[o][1]) * bflo(ed[o][1]));
            float s3 = bfr(bfhi(ex[o][1]) * bfhi(ed[o][1]));
            f32x4 res;
            res[0] = gg * (ss[0] + s0) + bfr(omg * bfr(sm[0]));
            res[1] = gg * (ss[1] + s1) + bfr(omg * bfr(sm[1]));
            res[2] = gg * (ss[2] + s2) + bfr(omg * bfr(sm[2]));
            res[3] = gg * (ss[3] + s3) + bfr(omg * bfr(sm[3]));
            *(f32x4*)(p.out + (size_t)t * DM + d) = res;
        }
    }
    STAMP(1, 3);
    STAMP_DRAIN(1, 4);
}

extern "C" void kernel_launch(void* const* d_in, const int* in_sizes, int n_in, void* d_out, int out_size,
                              void* d_ws, size_t ws_size, hipStream_t stream) {
    const unsigned short* x = (const unsigned short*)d_in[0];
    const unsigned short* lre = (const unsigned short*)d_in[1];
    const unsigned short* lim = (const unsigned short*)d_in[2];
    const unsigned short* bre = (const unsigned short*)d_in[3];
    const unsigned short* bim = (const unsigned short*)d_in[4];
    const unsigned short* cre = (const unsigned short*)d_in[5];
    const unsigned short* cim = (const unsigned short*)d_in[6];
    const unsigned short* ldt = (const unsigned short*)d_in[7];
    const unsigned short* dskip = (const unsigned short*)d_in[8];
    const unsigned short* w1 = (const unsigned short*)d_in[10];
    const unsigned short* w2 = (const unsigned short*)d_in[11];
    const unsigned short* gw = (const unsigned short*)d_in[12];
    char* ws = (char*)d_ws;
    float* out = (float*)d_out;

    K1Params p1{x, lre, lim, bre, bim, cre, cim, ldt, w1, w2, gw, ws};
    k0_kernel<<<K0_GRID, 512, 0, stream>>>((u32x4*)(ws + WS_GRAN));
    k1_kernel<<<K1_GRID, 512, 0, stream>>>(p1);
    K2Params p2{x, dskip, ws, out};
    k2_kernel<<<256, 512, 0, stream>>>(p2);
}
```

```cpp
#include <hip/hip_runtime.h>
#include <hip/hip_bf16.h>
#include <stdint.h>

typedef __attribute__((ext_vector_type(8))) __bf16 bf16x8;
typedef __attribute__((ext_vector_type(4))) __bf16 bf16x4;
typedef __attribute__((ext_vector_type(16))) float f32x16;
typedef __attribute__((ext_vector_type(4))) float f32x4;
typedef __attribute__((ext_vector_type(2))) float f32x2;
typedef __attribute__((ext_vector_type(4))) unsigned u32x4;
typedef __attribute__((ext_vector_type(2))) unsigned u32x2;
typedef __attribute__((address_space(3))) bf16x4* lds_bf16x4_ptr;

#define LSEQ 2048
#define DM 640
#define PN 640
#define FF 1728
#define KS_MLP (FF / 32)
#define KS_SSM (2 * PN / 32)
#define MT (LSEQ / 16)
#define NT (DM / 16)

#define WS_GRAN 0
#define WS_GRAN_SZ ((size_t)16 * PN * 2 * 8)
#define WS_HIDA (WS_GRAN + WS_GRAN_SZ)
#define WS_HIDA_SZ ((size_t)MT * KS_MLP * 1024)
#define WS_HA (WS_HIDA + WS_HIDA_SZ)
#define WS_HA_SZ ((size_t)MT * KS_SSM * 1024)
#define WS_W2T (WS_HA + WS_HA_SZ)
#define WS_W2T_SZ ((size_t)NT * KS_MLP * 1024)
#define WS_CT (WS_W2T + WS_W2T_SZ)
#define WS_CT_SZ ((size_t)NT * KS_SSM * 1024)
#define WS_G (WS_CT + WS_CT_SZ)

__device__ __forceinline__ float bf2f(unsigned short u) { return __uint_as_float(((unsigned)u) << 16); }
__device__ __forceinline__ float bflo(unsigned u) { return __uint_as_float(u << 16); }
__device__ __forceinline__ float bfhi(unsigned u) { return __uint_as_float(u & 0xffff0000u); }
__device__ __forceinline__ float bfr(float x) { return (float)(__bf16)x; }
__device__ __forceinline__ unsigned short f2bf(float x) {
    __bf16 b = (__bf16)x;
    return __builtin_bit_cast(unsigned short, b);
}
__device__ __forceinline__ unsigned pk2(float a, float b) { return f2bf(a) | ((unsigned)f2bf(b) << 16); }

__device__ __forceinline__ void sincos_acc(float x, float& s, float& c) {
    float kf = rintf(x * 0.63661977236758134f);
    float r = fmaf(kf, -1.5707962513e+00f, x);
    r = fmaf(kf, -7.5497894159e-08f, r);
    r = fmaf(kf, -5.3903029534e-15f, r);
    int k = (int)kf;
    float r2 = r * r;
    float sp = fmaf(r2, -1.9515295891e-04f, 8.3321608736e-03f);
    sp = fmaf(sp, r2, -1.6666654611e-01f);
    sp = fmaf(sp * r2, r, r);
    float cp = fmaf(r2, 2.443315711809948e-05f, -1.388731625493765e-03f);
    cp = fmaf(cp, r2, 4.166664568298827e-02f);
    cp = fmaf(cp * r2, r2, fmaf(-0.5f, r2, 1.0f));
    float ss = (k & 1) ? cp : sp;
    float cc = (k & 1) ? sp : cp;
    s = (k & 2) ? -ss : ss;
    c = ((k + 1) & 2) ? -cc : cc;
}

__device__ __forceinline__ void chan_lam(float lre, float lim, float ldt, float& Lr, float& Li) {
    float dt = bfr(expf(ldt));
    float a = dt * lre, b = dt * lim;
    float ea = expf(a);
    float sb, cb;
    sincos_acc(b, sb, cb);
    Lr = ea * cb;
    Li = ea * sb;
}
__device__ __forceinline__ void chan_consts(float lre, float lim, float ldt, float& Lr, float& Li,
                                            float& cr, float& ci) {
    float dt = bfr(expf(ldt));
    float a = dt * lre, b = dt * lim;
    float ea = expf(a);
    float sb, cb;
    sincos_acc(b, sb, cb);
    Lr = ea * cb;
    Li = ea * sb;
    float sh, chh;
    sincos_acc(0.5f * b, sh, chh);
    float nr = expm1f(a) * cb - 2.0f * sh * sh;
    float ni = Li;
    float den = lre * lre + lim * lim;
    float inv = 1.0f / den;
    cr = (nr * lre + ni * lim) * inv;
    ci = (ni * lre - nr * lim) * inv;
}

#define CMUL_R(ar, ai, br, bi) ((ar) * (br) - (ai) * (bi))
#define CMUL_I(ar, ai, br, bi) ((ar) * (bi) + (ai) * (br))

#define K1_SSM_BLOCKS 160
#define K1_MLP_BLOCKS 88
#define K1_GRID (K1_SSM_BLOCKS + K1_MLP_BLOCKS)
#define K0_GRID 20

__device__ __forceinline__ unsigned swz128(unsigned row, unsigned ch) {
    return row * 128u + ((ch ^ ((row >> 1) & 7u)) << 4);
}
__device__ __forceinline__ unsigned off_b(unsigned row, unsigned ch) {
    return 256u * row + 16u * (ch ^ (((row & 3u) << 2) | ((row >> 2) & 3u)));
}

struct K1Params {
    const unsigned short* x;
    const unsigned short* lre;
    const unsigned short* lim;
    const unsigned short* bre;
    const unsigned short* bim;
    const unsigned short* cre;
    const unsigned short* cim;
    const unsigned short* ldt;
    const unsigned short* w1;
    const unsigned short* w2;
    const unsigned short* gw;
    char* ws;
};

__device__ __forceinline__ bf16x4 tr_read_asm(unsigned lds_addr) {
    bf16x4 r;
    asm volatile("ds_read_b64_tr_b16 %0, %1" : "=v"(r) : "v"(lds_addr) : "memory");
    return r;
}
__device__ __forceinline__ bf16x8 lds_read128_asm(unsigned lds_addr) {
    bf16x8 r;
    asm volatile("ds_read_b128 %0, %1" : "=v"(r) : "v"(lds_addr) : "memory");
    return r;
}
template <int OFF>
__device__ __forceinline__ bf16x4 tr_read_off(unsigned lds_addr) {
    bf16x4 r;
    asm volatile("ds_read_b64_tr_b16 %0, %1 offset:%2" : "=v"(r) : "v"(lds_addr), "n"(OFF) : "memory");
    return r;
}
__device__ __forceinline__ unsigned opaque_s(unsigned v) {
    asm volatile("" : "+s"(v));
    return v;
}
__device__ __forceinline__ unsigned lds_addr_of(const char* ptr) {
    return (unsigned)(size_t)(__attribute__((address_space(3))) const char*)ptr;
}
#define K1_LDS_BYTES (3 * 53248 + 768)
#define RAW_BARRIER() do { asm volatile("s_waitcnt lgkmcnt(0)" ::: "memory"); __builtin_amdgcn_s_barrier(); } while (0)
#define GLDS16(SRC, DST) __builtin_amdgcn_global_load_lds((const unsigned*)(SRC), (unsigned*)(DST), 16, 0, 0)
#define SCHED_FENCE() __builtin_amdgcn_sched_barrier(0)

__device__ __forceinline__ void prep_slot(const K1Params& p, int slot, char* img, int lane) {
    const bool do_g = slot < 512;
    const int l16 = lane & 15;
    const int tg = (do_g ? slot : 0) * 4 + (lane >> 4);
    u32x4 xv[5], gv[5];
    if (do_g) {
#pragma unroll
        for (int i = 0; i < 5; ++i) {
            gv[i] = *(const u32x4*)(p.gw + (l16 + 16 * i) * 8);
            xv[i] = *(const u32x4*)(p.x + (size_t)tg * DM + (l16 + 16 * i) * 8);
        }
    }
    if (slot < KS_MLP * 10) {
        u32x4* w2t = (u32x4*)(p.ws + WS_W2T);
        const int ks = slot / 10, ng = slot % 10;
        u32x4 v[4];
#pragma unroll
        for (int i = 0; i < 4; ++i) {
            int idx = lane + 64 * i, row = idx >> 3, c8 = idx & 7;
            v[i] = *(const u32x4*)(p.w2 + (size_t)(32 * ks + row) * DM + ng * 64 + c8 * 8);
        }
#pragma unroll
        for (int i = 0; i < 4; ++i) {
            int idx = lane + 64 * i, row = idx >> 3, c8 = idx & 7;
            *(u32x4*)(img + row * 128 + c8 * 16) = v[i];
        }
        __builtin_amdgcn_fence(__ATOMIC_RELEASE, "wavefront");
        __builtin_amdgcn_wave_barrier();
        __builtin_amdgcn_fence(__ATOMIC_ACQUIRE, "wavefront");
        const int q = lane >> 4, qq = (lane & 15) >> 2, pp = lane & 3;
#pragma unroll
        for (int c = 0; c < 4; ++c) {
            int r0 = 16 * (q >> 1) + 4 * (q & 1);
            unsigned a0 = (r0 + qq) * 128 + c * 32 + pp * 8;
            unsigned a1 = a0 + 8 * 128;
            bf16x4 lo = __builtin_amdgcn_ds_read_tr16_b64_v4bf16((lds_bf16x4_ptr)(img + a0));
            bf16x4 hi = __builtin_amdgcn_ds_read_tr16_b64_v4bf16((lds_bf16x4_ptr)(img + a1));
            bf16x8 f = __builtin_shufflevector(lo, hi, 0, 1, 2, 3, 4, 5, 6, 7);
            w2t[((size_t)(ng * 4 + c) * KS_MLP + ks) * 64 + lane] = __builtin_bit_cast(u32x4, f);
        }
    } else if (slot < KS_MLP * 10 + 400) {
        u32x4* ct = (u32x4*)(p.ws + WS_CT);
        u32x4 re[2], im[2];
#pragma unroll
        for (int u = 0; u < 2; ++u) {
            int task = (slot - KS_MLP * 10) * 2 + u;
            int nt = task / 20, pg = task % 20;
            int d = nt * 16 + (lane & 15), pc = pg * 32 + (lane >> 4) * 8;
            re[u] = *(const u32x4*)(p.cre + (size_t)d * PN + pc);
            im[u] = *(const u32x4*)(p.cim + (size_t)d * PN + pc);
        }
#pragma unroll
        for (int u = 0; u < 2; ++u) {
            int task = (slot - KS_MLP * 10) * 2 + u;
            int nt = task / 20, pg = task % 20;
            int c = lane >> 4;
            int ks = pg * 2 + (c >> 1), q0 = 2 * (c & 1);
            u32x4 pa, pb;
            pa[0] = pk2(2.0f * bflo(re[u][0]), -2.0f * bflo(im[u][0]));
            pa[1] = pk2(2.0f * bfhi(re[u][0]), -2.0f * bfhi(im[u][0]));
            pa[2] = pk2(2.0f * bflo(re[u][1]), -2.0f * bflo(im[u][1]));
            pa[3] = pk2(2.0f * bfhi(re[u][1]), -2.0f * bfhi(im[u][1]));
            pb[0] = pk2(2.0f * bflo(re[u][2]), -2.0f * bflo(im[u][2]));
            pb[1] = pk2(2.0f * bfhi(re[u][2]), -2.0f * bfhi(im[u][2]));
            pb[2] = pk2(2.0f * bflo(re[u][3]), -2.0f * bflo(im[u][3]));
            pb[3] = pk2(2.0f * bfhi(re[u][3]), -2.0f * bfhi(im[u][3]));
            size_t o = ((size_t)nt * KS_SSM + ks) * 64 + (lane & 15);
            ct[o + 16 * q0] = pa;
            ct[o + 16 * (q0 + 1)] = pb;
        }
    }
    if (do_g) {
        float sum = 0.f;
#pragma unroll
        for (int i = 0; i < 5; ++i)
#pragma unroll
            for (int e = 0; e < 4; ++e) {
                sum += bfr(bflo(xv[i][e]) * bflo(gv[i][e]));
                sum += bfr(bfhi(xv[i][e]) * bfhi(gv[i][e]));
            }
#pragma unroll
        for (int o = 8; o > 0; o >>= 1) sum += __shfl_xor(sum, o);
        if (l16 == 0) {
            float z = bfr(sum);
            ((float*)(p.ws + WS_G))[tg] = bfr(1.0f / (1.0f + expf(-z)));
        }
    }
}

#define SS_STAGE 32768
#define SS_CONST_OFF (4 * SS_STAGE)
__device__ __forceinline__ void k1_ssm_tile(const K1Params& p, char* smem, int mtile, int ntile, int sbid) {
    const int tid = threadIdx.x;
    const int lane = tid & 63;
    const int wid = __builtin_amdgcn_readfirstlane(tid >> 6);
    const int t0 = mtile * 128;
    const int wt = wid >> 1, wn = wid & 1;

    const unsigned short* xs_src[2];
    const unsigned short* ws_src[2];
#pragma unroll
    for (int i = 0; i < 2; ++i) {
        unsigned row = (2 * wid + i) * 8 + (lane >> 3);
        unsigned chk = (lane & 7) ^ ((row >> 1) & 7);
        xs_src[i] = p.x + (size_t)(t0 + row) * DM + chk * 8;
        unsigned grp = row >> 5;
        const unsigned short* base = (grp & 1) ? p.bim : p.bre;
        ws_src[i] = base + (size_t)(ntile * 64 + (row & 31) + 32 * (grp >> 1)) * DM + chk * 8;
    }
#define SS_GLDS(KT)                                                                    \
    do {                                                                               \
        char* st_ = smem + ((KT) & 3) * SS_STAGE;                                      \
        const int ko_ = (KT) * 64;                                                     \
        GLDS16(xs_src[0] + ko_, st_ + (2 * wid) * 1024);                               \
        GLDS16(xs_src[1] + ko_, st_ + (2 * wid + 1) * 1024);                           \
        GLDS16(ws_src[0] + ko_, st_ + 16384 + (2 * wid) * 1024);                       \
        GLDS16(ws_src[1] + ko_, st_ + 16384 + (2 * wid + 1) * 1024);                   \
    } while (0)
    {
        const int cc = ntile * 64 + 2 * (lane & 31);
        __builtin_amdgcn_global_load_lds((const unsigned*)(p.lre + cc), (unsigned*)(smem + SS_CONST_OFF), 4, 0, 0);
        __builtin_amdgcn_global_load_lds((const unsigned*)(p.lim + cc), (unsigned*)(smem + SS_CONST_OFF + 256), 4, 0, 0);
        __builtin_amdgcn_global_load_lds((const unsigned*)(p.ldt + cc), (unsigned*)(smem + SS_CONST_OFF + 512), 4, 0, 0);
    }
    SS_GLDS(0);
    SS_GLDS(1);
    SS_GLDS(2);
    SS_GLDS(3);

    f32x16 accR, accI;
#pragma unroll
    for (int r = 0; r < 16; ++r) { accR[r] = 0.f; accI[r] = 0.f; }
    const unsigned l31 = lane & 31, lh = lane >> 5;
    const int ch = ntile * 64 + wn * 32 + l31;
    const unsigned xa0 = wt * 32 + l31;
    const unsigned ba0 = wn * 64 + l31, ba1 = ba0 + 32;
    const unsigned sbase = lds_addr_of(smem);

    unsigned sxo[4], sbo0[4], sbo1[4];
#pragma unroll
    for (int q = 0; q < 4; ++q) {
        sxo[q] = swz128(xa0, 2 * q + lh);
        sbo0[q] = 16384 + swz128(ba0, 2 * q + lh);
        sbo1[q] = 16384 + swz128(ba1, 2 * q + lh);
    }
#define SS_FRAGS(F, KT, S)                                                             \
    do {                                                                               \
        const unsigned xb_ = opaque_s(sbase + ((KT) & 3) * SS_STAGE);                  \
        F[0] = lds_read128_asm(xb_ + sxo[S]);                                          \
        F[1] = lds_read128_asm(xb_ + sbo0[S]);                                         \
        F[2] = lds_read128_asm(xb_ + sbo1[S]);                                         \
    } while (0)
#define SS_WAIT(F, N) asm volatile("s_waitcnt lgkmcnt(" #N ")" : "+v"(F[0]), "+v"(F[1]), "+v"(F[2])::"memory")
#define SS_MMA(F)                                                                      \
    do {                                                                               \
        accR = __builtin_amdgcn_mfma_f32_32x32x16_bf16(F[0], F[1], accR, 0, 0, 0);     \
        accI = __builtin_amdgcn_mfma_f32_32x32x16_bf16(F[0], F[2], accI, 0, 0, 0);     \
    } while (0)
    bf16x8 fA[3], fB[3];
#define SS_STEP(KT)                                                                    \
    do {                                                                               \
        SS_WAIT(fA, 3); SS_MMA(fA); SCHED_FENCE();                                     \
        SS_FRAGS(fA, KT, 2); SCHED_FENCE();                                            \
        SS_WAIT(fB, 3); SS_MMA(fB); SCHED_FENCE();                                     \
        SS_FRAGS(fB, KT, 3); SCHED_FENCE();                                            \
        SS_WAIT(fA, 3); SS_MMA(fA); SCHED_FENCE();                                     \
        if ((KT) + 1 < 10) {                                                           \
            if ((KT) + 3 < 10) asm volatile("s_waitcnt vmcnt(8)" ::: "memory");        \
            else if ((KT) + 2 < 10) asm volatile("s_waitcnt vmcnt(4)" ::: "memory");   \
            else asm volatile("s_waitcnt vmcnt(0)" ::: "memory");                      \
            RAW_BARRIER();                                                             \
            if ((KT) + 4 < 10) SS_GLDS((KT) + 4);                                      \
            SS_FRAGS(fA, (KT) + 1, 0); SCHED_FENCE();                                  \
            SS_WAIT(fB, 3);                                                            \
        } else {                                                                       \
            SS_WAIT(fB, 0);                                                            \
        }                                                                              \
        SS_MMA(fB); SCHED_FENCE();                                                     \
        if ((KT) + 1 < 10) SS_FRAGS(fB, (KT) + 1, 1);                                  \
        SCHED_FENCE();                                                                 \
    } while (0)
    asm volatile("s_waitcnt vmcnt(12)" ::: "memory");
    RAW_BARRIER();
    SS_FRAGS(fA, 0, 0);
    SS_FRAGS(fB, 0, 1);
    SCHED_FENCE();
    float Lr, Li, cr, ci;
    {
        const unsigned short* cst = (const unsigned short*)(smem + SS_CONST_OFF);
        const int cl0 = wn * 32 + l31;
        chan_consts(bf2f(cst[cl0]), bf2f(cst[128 + cl0]), bf2f(cst[256 + cl0]), Lr, Li, cr, ci);
    }
    const float L2r = CMUL_R(Lr, Li, Lr, Li), L2i = CMUL_I(Lr, Li, Lr, Li);
    const float L3r = CMUL_R(L2r, L2i, Lr, Li), L3i = CMUL_I(L2r, L2i, Lr, Li);
    const float L4r = CMUL_R(L2r, L2i, L2r, L2i), L4i = CMUL_I(L2r, L2i, L2r, L2i);
    float Pr = L4r, Pi = L4i;
#pragma unroll
    for (int i = 0; i < 3; ++i) { float nr = Pr * Pr - Pi * Pi, ni = 2.f * Pr * Pi; Pr = nr; Pi = ni; }
    SCHED_FENCE();
    SS_STEP(0); SS_STEP(1); SS_STEP(2); SS_STEP(3); SS_STEP(4);
    SS_STEP(5); SS_STEP(6); SS_STEP(7); SS_STEP(8); SS_STEP(9);
#undef SS_STEP
#undef SS_MMA
#undef SS_WAIT
#undef SS_FRAGS
#undef SS_GLDS

    f32x16 vr, vi;
#pragma unroll
    for (int r = 0; r < 16; ++r) {
        vr[r] = cr * accR[r] - ci * accI[r];
        vi[r] = cr * accI[r] + ci * accR[r];
    }
#pragma unroll
    for (int j = 0; j < 4; ++j)
#pragma unroll
        for (int i = 1; i < 4; ++i) {
            int b = 4 * j + i;
            float pr = vr[b - 1], pi = vi[b - 1];
            vr[b] += Lr * pr - Li * pi;
            vi[b] += Lr * pi + Li * pr;
        }
    float E0r[4], E0i[4], E1r[4], E1i[4];
#pragma unroll
    for (int g = 0; g < 4; ++g) {
        unsigned er = __float_as_uint(vr[4 * g + 3]), ei = __float_as_uint(vi[4 * g + 3]);
        auto sr = __builtin_amdgcn_permlane32_swap(er, er, false, false);
        auto si = __builtin_amdgcn_permlane32_swap(ei, ei, false, false);
        E0r[g] = __uint_as_float(sr[0]); E0i[g] = __uint_as_float(si[0]);
        E1r[g] = __uint_as_float(sr[1]); E1i[g] = __uint_as_float(si[1]);
    }
    float aR = 0.f, aI = 0.f;
#pragma unroll
    for (int g = 0; g < 4; ++g) {
        float c1r = L4r * aR - L4i * aI + E0r[g], c1i = L4r * aI + L4i * aR + E0i[g];
        aR = L4r * c1r - L4i * c1i + E1r[g];
        aI = L4r * c1i + L4i * c1r + E1i[g];
    }
    f32x2* aggL = (f32x2*)smem;
    f32x2* hinL = (f32x2*)(smem + 2048);
    const int cl = wn * 32 + l31;
    __syncthreads();
    if (lh == 0) aggL[wt * 64 + cl] = (f32x2){aR, aI};
    __syncthreads();
    float bR = 0.f, bI = 0.f, inR = 0.f, inI = 0.f;
#pragma unroll
    for (int w = 0; w < 4; ++w) {
        if (w == wt) { inR = bR; inI = bI; }
        f32x2 a = aggL[w * 64 + cl];
        float nr = Pr * bR - Pi * bI + a[0], ni = Pr * bI + Pi * bR + a[1];
        bR = nr; bI = ni;
    }
    if (wt == 0) {
        unsigned long long* gran = (unsigned long long*)(p.ws + WS_GRAN);
        const float myv = lh ? bI : bR;
        __hip_atomic_store(&gran[((size_t)mtile * PN + ch) * 2 + lh],
                           (1ull << 32) | (unsigned long long)__float_as_uint(myv), __ATOMIC_RELAXED,
                           __HIP_MEMORY_SCOPE_AGENT);
        float Qr = Pr * Pr - Pi * Pi, Qi = 2.f * Pr * Pi;
        { float nr = Qr * Qr - Qi * Qi, ni = 2.f * Qr * Qi; Qr = nr; Qi = ni; }
        unsigned val[15];
        for (unsigned spin = 0; spin < (1u << 24); ++spin) {
            bool ok = true;
#pragma unroll
            for (int m = 0; m < 15; ++m) {
                if (m < mtile) {
                    unsigned long long x = __hip_atomic_load(&gran[((size_t)m * PN + ch) * 2 + lh], __ATOMIC_RELAXED,
                                                             __HIP_MEMORY_SCOPE_AGENT);
                    val[m] = (unsigned)x;
                    ok = ok && ((x >> 32) == 1ull);
                }
            }
            if (__all(ok)) break;
            __builtin_amdgcn_s_sleep(1);
        }
        float hR = 0.f, hI = 0.f;
#pragma unroll
        for (int m = 0; m < 15; ++m) {
            if (m < mtile) {
                auto sw = __builtin_amdgcn_permlane32_swap(val[m], val[m], false, false);
                float er = __uint_as_float(sw[0]), ei = __uint_as_float(sw[1]);
                float nr = Qr * hR - Qi * hI + er, ni = Qr * hI + Qi * hR + ei;
                hR = nr; hI = ni;
            }
        }
        if (lh == 0) hinL[cl] = (f32x2){hR, hI};
    } else {
        prep_slot(p, sbid * 6 + (wid - 2), smem + 49152 + wid * 4096, lane);
    }
    __syncthreads();
    float cR, cI;
    {
        f32x2 hb = hinL[cl];
        float hR = hb[0], hI = hb[1];
#pragma unroll
        for (int w = 0; w < 3; ++w)
            if (w < wt) { float nr = Pr * hR - Pi * hI, ni = Pr * hI + Pi * hR; hR = nr; hI = ni; }
        cR = hR + inR; cI = hI + inI;
    }
#pragma unroll
    for (int j = 0; j < 4; ++j) {
        float c0r = cR, c0i = cI;
        float c1r = L4r * cR - L4i * cI + E0r[j];
        float c1i = L4r * cI + L4i * cR + E0i[j];
        cR = L4r * c1r - L4i * c1i + E1r[j];
        cI = L4r * c1i + L4i * c1r + E1i[j];
        float inr = lh ? c1r : c0r, ini = lh ? c1i : c0i;
        vr[4 * j + 0] += Lr * inr - Li * ini;
        vi[4 * j + 0] += Lr * ini + Li * inr;
        vr[4 * j + 1] += L2r * inr - L2i * ini;
        vi[4 * j + 1] += L2r * ini + L2i * inr;
        vr[4 * j + 2] += L3r * inr - L3i * ini;
        vi[4 * j + 2] += L3r * ini + L3i * inr;
        vr[4 * j + 3] += L4r * inr - L4i * ini;
        vi[4 * j + 3] += L4r * ini + L4i * inr;
    }
    char* tile = smem + 4096 + wid * 4608;
#pragma unroll
    for (int r = 0; r < 16; ++r) {
        int tl = (r & 3) + 8 * (r >> 2) + 4 * lh;
        *(unsigned*)(tile + tl * 144 + l31 * 4) = pk2(vr[r], vi[r]);
    }
    __builtin_amdgcn_fence(__ATOMIC_RELEASE, "wavefront");
    __builtin_amdgcn_wave_barrier();
    __builtin_amdgcn_fence(__ATOMIC_ACQUIRE, "wavefront");
    u32x4* ha = (u32x4*)(p.ws + WS_HA);
#pragma unroll
    for (int mq = 0; mq < 2; ++mq)
#pragma unroll
        for (int k2 = 0; k2 < 2; ++k2) {
            u32x4 v = *(const u32x4*)(tile + (mq * 16 + (lane & 15)) * 144 + (k2 * 4 + (lane >> 4)) * 16);
            int mt = ((t0 + wt * 32) >> 4) + mq;
            int ks = ((ntile * 64 + wn * 32) >> 4) + k2;
            ha[((size_t)mt * KS_SSM + ks) * 64 + lane] = v;
        }
}

#define ML_STAGE 53248
__device__ __forceinline__ void k1_mlp_tile(const K1Params& p, char* smem, int mtile, int ntile) {
    const int tid = threadIdx.x;
    const int lane = tid & 63;
    const int wid = __builtin_amdgcn_readfirstlane(tid >> 6);
    const int t0 = mtile * 256;

    const unsigned short* xs_src[4];
#pragma unroll
    for (int i = 0; i < 4; ++i) {
        unsigned row = (4 * wid + i) * 8 + (lane >> 3);
        unsigned chk = (lane & 7) ^ ((row >> 1) & 7);
        xs_src[i] = p.x + (size_t)(t0 + row) * DM + chk * 8;
    }
    const unsigned short* wa_src[2];
#pragma unroll
    for (int i = 0; i < 2; ++i) {
        unsigned row = (2 * wid + i) * 4 + (lane >> 4);
        unsigned chk = (lane & 15) ^ (((row & 3u) << 2) | ((row >> 2) & 3u));
        wa_src[i] = p.w1 + (size_t)row * FF + ntile * 160 + chk * 8;
    }
    const unsigned short* wb_src;
    {
        unsigned row = (wid & 3) * 16 + (lane >> 2);
        int n = ntile * 160 + 128 + (lane & 3) * 8;
        if (n > FF - 8) n = FF - 8;
        wb_src = p.w1 + (size_t)row * FF + n;
    }
#define ML_GLDS(KT)                                                                          \
    do {                                                                                     \
        char* st_ = smem + ((KT) % 3) * ML_STAGE;                                            \
        const int ko_ = (KT) * 64;                                                           \
        _Pragma("unroll") for (int i = 0; i < 4; ++i) GLDS16(xs_src[i] + ko_, st_ + (4 * wid + i) * 1024); \
        _Pragma("unroll") for (int i = 0; i < 2; ++i) GLDS16(wa_src[i] + (size_t)ko_ * FF, st_ + 32768 + (2 * wid + i) * 1024); \
        GLDS16(wb_src + (size_t)ko_ * FF, st_ + 49152 + (wid & 3) * 1024);                   \
    } while (0)
    ML_GLDS(0);
    ML_GLDS(1);
    ML_GLDS(2);

    f32x16 acc[5];
#pragma unroll
    for (int i = 0; i < 5; ++i)
#pragma unroll
        for (int r = 0; r < 16; ++r) acc[i][r] = 0.f;
    const unsigned l31 = lane & 31, lh = lane >> 5;
    const unsigned xa0 = wid * 32 + l31;
    const unsigned tq = (lane & 15) >> 2, tp = lane & 3, tblk = (lane >> 4) & 1;
    const unsigned sbase = lds_addr_of(smem);

    unsigned mxo[4], mwo[8], mbo;
#pragma unroll
    for (int q = 0; q < 4; ++q) mxo[q] = swz128(xa0, 2 * q + lh);
#pragma unroll
    for (int c = 0; c < 4; ++c) {
        mwo[2 * c] = 32768 + off_b(8 * lh + 0 + tq, 4 * c + 2 * tblk + (tp >> 1)) + 8 * (tp & 1);
        mwo[2 * c + 1] = 32768 + off_b(8 * lh + 4 + tq, 4 * c + 2 * tblk + (tp >> 1)) + 8 * (tp & 1);
    }
    mbo = 49152 + (8 * lh + tq) * 64 + tblk * 32 + tp * 8;
#define ML_FRAGS(X, T, KT, S)                                                                \
    do {                                                                                     \
        const unsigned xb_ = opaque_s(sbase + ((KT) % 3) * ML_STAGE);                        \
        X = lds_read128_asm(xb_ + mxo[S]);                                                   \
        _Pragma("unroll") for (int c = 0; c < 8; ++c) T[c] = tr_read_off<4096 * (S)>(xb_ + mwo[c]); \
        T[8] = tr_read_off<1024 * (S)>(xb_ + mbo);                                           \
        T[9] = tr_read_off<1024 * (S) + 256>(xb_ + mbo);                                     \
    } while (0)
#define ML_WAIT(X, T, N)                                                                     \
    asm volatile("s_waitcnt lgkmcnt(" #N ")" : "+v"(X), "+v"(T[0]), "+v"(T[1]), "+v"(T[2]), "+v"(T[3]), "+v"(T[4]), \
                 "+v"(T[5]), "+v"(T[6]), "+v"(T[7]), "+v"(T[8]), "+v"(T[9])::"memory")
#define ML_MMA(X, T)                                                                         \
    do {                                                                                     \
        _Pragma("unroll") for (int c = 0; c < 5; ++c) {                                      \
            bf16x8 wf_ = __builtin_shufflevector(T[2 * c], T[2 * c + 1], 0, 1, 2, 3, 4, 5, 6, 7); \
            acc[c] = __builtin_amdgcn_mfma_f32_32x32x16_bf16(wf_, X, acc[c], 0, 0, 0);       \
        }                                                                                    \
    } while (0)
    bf16x8 xA, xB;
    bf16x4 tA[10], tB[10];
#define ML_STEP(KT)                                                                          \
    do {                                                                                     \
        ML_WAIT(xA, tA, 11); ML_MMA(xA, tA); SCHED_FENCE();                                  \
        ML_FRAGS(xA, tA, KT, 2); SCHED_FENCE();                                              \
        ML_WAIT(xB, tB, 11); ML_MMA(xB, tB); SCHED_FENCE();                                  \
        ML_FRAGS(xB, tB, KT, 3); SCHED_FENCE();                                              \
        ML_WAIT(xA, tA, 11); ML_MMA(xA, tA); SCHED_FENCE();                                  \
        if ((KT) + 1 < 10) {                                                                 \
            if ((KT) + 2 < 10) asm volatile("s_waitcnt vmcnt(7)" ::: "memory");              \
            else asm volatile("s_waitcnt vmcnt(0)" ::: "memory");                            \
            RAW_BARRIER();                                                                   \
            if ((KT) + 3 < 10) ML_GLDS((KT) + 3);                                            \
            ML_FRAGS(xA, tA, (KT) + 1, 0); SCHED_FENCE();                                    \
            ML_WAIT(xB, tB, 11);                                                             \
        } else {                                                                             \
            ML_WAIT(xB, tB, 0);                                                              \
        }                                                                                    \
        ML_MMA(xB, tB); SCHED_FENCE();                                                       \
        if ((KT) + 1 < 10) ML_FRAGS(xB, tB, (KT) + 1, 1);                                    \
        SCHED_FENCE();                                                                       \
    } while (0)
    asm volatile("s_waitcnt vmcnt(14)" ::: "memory");
    RAW_BARRIER();
    ML_FRAGS(xA, tA, 0, 0);
    ML_FRAGS(xB, tB, 0, 1);
    SCHED_FENCE();
    ML_STEP(0); ML_STEP(1); ML_STEP(2); ML_STEP(3); ML_STEP(4);
    ML_STEP(5); ML_STEP(6); ML_STEP(7); ML_STEP(8); ML_STEP(9);
#undef ML_STEP
#undef ML_MMA
#undef ML_WAIT
#undef ML_FRAGS
#undef ML_GLDS
    u32x4* hid = (u32x4*)(p.ws + WS_HIDA);
    const int t = t0 + wid * 32 + l31;
    const int mt = t >> 4;
#pragma unroll
    for (int fi = 0; fi < 5; ++fi) {
        int f0 = ntile * 160 + fi * 32;
        if (f0 < FF) {
            int ks = f0 >> 5;
#pragma unroll
            for (int half = 0; half < 2; ++half) {
                u32x4 pk;
#pragma unroll
                for (int j = 0; j < 4; ++j) {
                    float v0 = acc[fi][8 * half + 2 * j];
                    float v1 = acc[fi][8 * half + 2 * j + 1];
                    v0 = v0 > 0.f ? v0 : 0.f;
                    v1 = v1 > 0.f ? v1 : 0.f;
                    pk[j] = pk2(v0, v1);
                }
                int q = 2 * half + lh;
                hid[((size_t)mt * KS_MLP + ks) * 64 + (t & 15) + 16 * q] = pk;
            }
        }
    }
}

__global__ __launch_bounds__(512, 2) void k1_kernel(K1Params p) {
    __shared__ __attribute__((aligned(16))) char smem[K1_LDS_BYTES];
    int bid = blockIdx.x;
    if (bid < K1_SSM_BLOCKS) {
        int xcd = bid & 7, idx = bid >> 3;
        k1_ssm_tile(p, smem, (xcd >> 1) * 4 + (idx & 3), (xcd & 1) * 5 + (idx >> 2), bid);
    } else {
        bid -= K1_SSM_BLOCKS;
        k1_mlp_tile(p, smem, bid & 7, bid >> 3);
    }
}

__global__ __launch_bounds__(512) void k0_kernel(u32x4* g) {
    g[blockIdx.x * 512 + threadIdx.x] = (u32x4){0u, 0u, 0u, 0u};
}

struct Frag9 { u32x4 a[4]; u32x4 b[5]; };

__device__ __forceinline__ void g2_load(Frag9& f, const char* __restrict__ ap, const char* __restrict__ bp,
                                        unsigned voff, int KS, int ks) {
#pragma unroll
    for (int i = 0; i < 4; ++i) f.a[i] = *(const u32x4*)(ap + (size_t)((i * KS + ks) * 1024) + voff);
#pragma unroll
    for (int j = 0; j < 5; ++j) f.b[j] = *(const u32x4*)(bp + (size_t)((j * KS + ks) * 1024) + voff);
    __builtin_amdgcn_sched_barrier(0);
}
__device__ __forceinline__ void g2_mma(const Frag9& f, f32x4 (&acc)[4][5]) {
#pragma unroll
    for (int i = 0; i < 4; ++i)
#pragma unroll
        for (int j = 0; j < 5; ++j)
            acc[i][j] = __builtin_amdgcn_mfma_f32_16x16x32_bf16(__builtin_bit_cast(bf16x8, f.b[j]),
                                                                __builtin_bit_cast(bf16x8, f.a[i]), acc[i][j], 0, 0, 0);
    __builtin_amdgcn_sched_barrier(0);
}

template <int KS, int KSTRIDE, int NSTEPS>
__device__ __forceinline__ void g2_run(const char* __restrict__ ap, const char* __restrict__ bp, unsigned voff,
                                       int k0, f32x4 (&acc)[4][5]) {
    Frag9 f0, f1, f2, f3;
    g2_load(f0, ap, bp, voff, KS, k0);
    g2_load(f1, ap, bp, voff, KS, k0 + KSTRIDE);
    g2_load(f2, ap, bp, voff, KS, k0 + 2 * KSTRIDE);
    g2_load(f3, ap, bp, voff, KS, k0 + 3 * KSTRIDE);
#pragma unroll
    for (int st = 0; st < NSTEPS; ++st) {
        const int kn = k0 + (st + 4) * KSTRIDE;
        if ((st & 3) == 0) { g2_mma(f0, acc); if (st + 4 < NSTEPS) g2_load(f0, ap, bp, voff, KS, kn); }
        if ((st & 3) == 1) { g2_mma(f1, acc); if (st + 4 < NSTEPS) g2_load(f1, ap, bp, voff, KS, kn); }
        if ((st & 3) == 2) { g2_mma(f2, acc); if (st + 4 < NSTEPS) g2_load(f2, ap, bp, voff, KS, kn); }
        if ((st & 3) == 3) { g2_mma(f3, acc); if (st + 4 < NSTEPS) g2_load(f3, ap, bp, voff, KS, kn); }
    }
}

struct K2Params {
    const unsigned short* x;
    const unsigned short* dskip;
    char* ws;
    float* out;
};

__global__ __launch_bounds__(512, 2) void k2_kernel(K2Params p) {
    __shared__ __attribute__((aligned(16))) float red[20 * 7 * 256];
    const int bid = blockIdx.x;
    const int xcd = bid & 7, idx = bid >> 3;
    const int mb = (xcd >> 1) * 8 + (idx >> 2), nb = (xcd & 1) * 4 + (idx & 3);
    const int lane = threadIdx.x & 63;
    const int wid = __builtin_amdgcn_readfirstlane(threadIdx.x >> 6);
    const unsigned voff = lane * 16;
    f32x4 acc[4][5];
#pragma unroll
    for (int i = 0; i < 4; ++i)
#pragma unroll
        for (int j = 0; j < 5; ++j) acc[i][j] = (f32x4){0.f, 0.f, 0.f, 0.f};
    if (wid < 5) {
        const char* ap = p.ws + WS_HIDA + (size_t)(mb * 4) * KS_MLP * 1024;
        const char* bp = p.ws + WS_W2T + (size_t)(nb * 5) * KS_MLP * 1024;
        if (wid < 4) g2_run<KS_MLP, 5, 11>(ap, bp, voff, wid, acc);
        else g2_run<KS_MLP, 5, 10>(ap, bp, voff, wid, acc);
    } else {
        const char* ap = p.ws + WS_HA + (size_t)(mb * 4) * KS_SSM * 1024;
        const char* bp = p.ws + WS_CT + (size_t)(nb * 5) * KS_SSM * 1024;
        if (wid == 5) g2_run<KS_SSM, 3, 14>(ap, bp, voff, 0, acc);
        else g2_run<KS_SSM, 3, 13>(ap, bp, voff, wid - 5, acc);
    }
    const float* g = (const float*)(p.ws + WS_G);
    u32x2 ex[3], ed[3];
    float eg[3];
#pragma unroll
    for (int o = 0; o < 3; ++o) {
        int tau = wid + 8 * o;
        if (tau > 19) tau = 19;
        int i = tau / 5, j = tau % 5;
        int t = mb * 64 + i * 16 + (lane & 15);
        int d = nb * 80 + j * 16 + (lane >> 4) * 4;
        eg[o] = g[t];
        ex[o] = *(const u32x2*)(p.x + (size_t)t * DM + d);
        ed[o] = *(const u32x2*)(p.dskip + d);
    }
    f32x4* r4 = (f32x4*)red;
#pragma unroll
    for (int tau = 0; tau < 20; ++tau) {
        const int owner = tau % 8;
        if (wid != owner) {
            int wslot = wid < owner ? wid : wid - 1;
            r4[(tau * 7 + wslot) * 64 + lane] = acc[tau / 5][tau % 5];
        }
    }
    __syncthreads();
#pragma unroll
    for (int tau = 0; tau < 20; ++tau) {
        if (wid == tau % 8) {
            const int o = tau / 8, owner = tau % 8;
            f32x4 sm = (f32x4){0.f, 0.f, 0.f, 0.f}, ss = (f32x4){0.f, 0.f, 0.f, 0.f};
            if (owner < 5) sm = acc[tau / 5][tau % 5]; else ss = acc[tau / 5][tau % 5];
#pragma unroll
            for (int w = 0; w < 7; ++w) {
                const int writer = w < owner ? w : w + 1;
                f32x4 v = r4[(tau * 7 + w) * 64 + lane];
                if (writer < 5) sm += v; else ss += v;
            }
            const int i = tau / 5, j = tau % 5;
            int t = mb * 64 + i * 16 + (lane & 15);
            int d = nb * 80 + j * 16 + (lane >> 4) * 4;
            const float gg = eg[o];
            const float omg = bfr(1.0f - gg);
            float s0 = bfr(bflo(ex[o][0]) * bflo(ed[o][0]));
            float s1 = bfr(bfhi(ex[o][0]) * bfhi(ed[o][0]));
            float s2 = bfr(bflo(ex[o][1]) * bflo(ed[o][1]));
            float s3 = bfr(bfhi(ex[o][1]) * bfhi(ed[o][1]));
            f32x4 res;
            res[0] = gg * (ss[0] + s0) + bfr(omg * bfr(sm[0]));
            res[1] = gg * (ss[1] + s1) + bfr(omg * bfr(sm[1]));
            res[2] = gg * (ss[2] + s2) + bfr(omg * bfr(sm[2]));
            res[3] = gg * (ss[3] + s3) + bfr(omg * bfr(sm[3]));
            *(f32x4*)(p.out + (size_t)t * DM + d) = res;
        }
    }
}

extern "C" void kernel_launch(void* const* d_in, const int* in_sizes, int n_in, void* d_out, int out_size,
                              void* d_ws, size_t ws_size, hipStream_t stream) {
    const unsigned short* x = (const unsigned short*)d_in[0];
    const unsigned short* lre = (const unsigned short*)d_in[1];
    const unsigned short* lim = (const unsigned short*)d_in[2];
    const unsigned short* bre = (const unsigned short*)d_in[3];
    const unsigned short* bim = (const unsigned short*)d_in[4];
    const unsigned short* cre = (const unsigned short*)d_in[5];
    const unsigned short* cim = (const unsigned short*)d_in[6];
    const unsigned short* ldt = (const unsigned short*)d_in[7];
    const unsigned short* dskip = (const unsigned short*)d_in[8];
    const unsigned short* w1 = (const unsigned short*)d_in[10];
    const unsigned short* w2 = (const unsigned short*)d_in[11];
    const unsigned short* gw = (const unsigned short*)d_in[12];
    char* ws = (char*)d_ws;
    float* out = (float*)d_out;

    K1Params p1{x, lre, lim, bre, bim, cre, cim, ldt, w1, w2, gw, ws};
    k0_kernel<<<K0_GRID, 512, 0, stream>>>((u32x4*)(ws + WS_GRAN));
    k1_kernel<<<K1_GRID, 512, 0, stream>>>(p1);
    K2Params p2{x, dskip, ws, out};
    k2_kernel<<<256, 512, 0, stream>>>(p2);
}
```
